```python
import torch

def _softmax(x, axis=-1):
    return torch.softmax(x, dim=axis)

def _gelu(x):
    return torch.nn.functional.gelu(x, approximate='tanh')

def _silu(x):
    return torch.nn.functional.silu(x)

def _sigmoid(x):
    return torch.sigmoid(x)

def _layernorm(x, axis=-1, eps=1e-05):
    m = x.mean(dim=axis, keepdim=True)
    v = x.var(dim=axis, keepdim=True, unbiased=False)
    return (x - m) / torch.sqrt(v + eps)

def _scatter_add(tgt, idx, src, axis=0):
    return tgt.clone().index_add_(axis, idx, src)

def _segment_sum(src, seg, n):
    out = torch.zeros((n,) + src.shape[1:], dtype=src.dtype)
    return out.index_add_(0, seg, src)

def _pad(x, pw):
    flat = tuple((v for pair in reversed(pw) for v in pair))
    return torch.nn.functional.pad(x, flat)
kernel_name = 'adaLN_cond__spectral_gfnet__glu_gated_bilinear__prompt'

def setup_inputs():

    _w = lambda a: (lambda t: t.to(torch.bfloat16) if t.is_floating_point() else t)(torch.as_tensor(a))

    def _A_setup():
        B, S, D = (1, 2048, 512)
        Df = D // 2 + 1
        return {'x': _w((lambda *s: torch.randn(*s, dtype=torch.bfloat16) * 0.05)(B, S, D)), 'w_r': _w((lambda *s: torch.randn(*s, dtype=torch.bfloat16) * 3.2)(S, Df) * 0.02), 'w_i': _w((lambda *s: torch.randn(*s, dtype=torch.bfloat16) * 3.2)(S, Df) * 0.02)}
    _A_in = _A_setup()

    def _B_setup():
        B, S, D, F = (1, 2048, 512, 1376)
        return {'x': _w((lambda *s: torch.randn(*s, dtype=torch.bfloat16) * 0.05)(B, S, D)), 'w_gate': _w((lambda *s: torch.randn(*s, dtype=torch.bfloat16) * 0.4)(D, F) * D ** (-0.5)), 'w_up': _w((lambda *s: torch.randn(*s, dtype=torch.bfloat16) * 0.4)(D, F) * D ** (-0.5)), 'w_down': _w((lambda *s: torch.randn(*s, dtype=torch.bfloat16) * 0.4)(F, D) * F ** (-0.5))}
    _B_in = _B_setup()
    return {'A_x': _A_in['x'], 'A_w_r': _A_in['w_r'], 'A_w_i': _A_in['w_i'], 'B_x': _B_in['x'], 'B_w_gate': _B_in['w_gate'], 'B_w_up': _B_in['w_up'], 'B_w_down': _B_in['w_down'], 'c': _w((lambda *s: torch.randn(*s, dtype=torch.bfloat16) * 0.1)(1, 512) * 8.0), 'w_mod': _w((lambda *s: torch.randn(*s, dtype=torch.bfloat16) * 0.1)(512, 6 * 512) * 512 ** (-0.5) * 8.0)}

def reference(A_x, A_w_r, A_w_i, B_x, B_w_gate, B_w_up, B_w_down, c, w_mod):

    def _A_ref(x, w_r, w_i):
        B, S, D = x.shape
        X = (lambda x, axes=(-2, -1): torch.fft.rfft2(x.float(), dim=axes))(x, axes=(-2, -1))
        W = w_r + 1j * w_i
        X = X * W
        y = (lambda x, s, axes=(-2, -1): torch.fft.irfft2(x, s=s, dim=axes).to(torch.bfloat16))(X, (S, D), axes=(-2, -1))
        return y

    def _B_ref(x, w_gate, w_up, w_down):
        g = torch.einsum('bsd,df->bsf', x, w_gate)
        u = torch.einsum('bsd,df->bsf', x, w_up)
        h = g * u
        return torch.einsum('bsf,fd->bsd', h, w_down)
    x = A_x
    mod = (_silu(c) @ w_mod).reshape(c.shape[0], 6, 1, -1)
    sa, ca, ga, sb, cb, gb = (mod[:, 0].clone(), mod[:, 1].clone(), mod[:, 2].clone(), mod[:, 3].clone(), mod[:, 4].clone(), mod[:, 5].clone())
    h = _layernorm(x, axis=-1) * (1 + ca) + sa
    x = x + ga * _A_ref(h, A_w_r, A_w_i)
    h = _layernorm(x, axis=-1) * (1 + cb) + sb
    x = x + gb * _B_ref(h, B_w_gate, B_w_up, B_w_down)
    return x
```

```cpp
#include <hip/hip_runtime.h>
#include <hip/hip_bf16.h>
#include <stdint.h>

typedef unsigned short u16;
typedef _Float16 h2 __attribute__((ext_vector_type(2)));
typedef _Float16 h4 __attribute__((ext_vector_type(4)));
typedef _Float16 h8 __attribute__((ext_vector_type(8)));
typedef __bf16 b2 __attribute__((ext_vector_type(2)));
typedef __bf16 b8 __attribute__((ext_vector_type(8)));
typedef float f2 __attribute__((ext_vector_type(2)));
typedef float f4 __attribute__((ext_vector_type(4)));
typedef float f16v __attribute__((ext_vector_type(16)));
typedef unsigned u2 __attribute__((ext_vector_type(2)));
typedef unsigned u4 __attribute__((ext_vector_type(4)));
typedef unsigned u4a __attribute__((ext_vector_type(4), aligned(4)));
typedef short s4 __attribute__((ext_vector_type(4)));
#define DI __device__ __forceinline__
#define LDS3 __attribute__((address_space(3)))

constexpr int FH = 1376;
constexpr size_t OFF_MOD = 0;
constexpr size_t OFF_MMAX = 12288;
constexpr size_t OFF_BETA = 12544;
constexpr size_t OFF_Y = 16384;
constexpr size_t OFF_WP = OFF_Y + 2097152;
constexpr size_t OFF_V = OFF_WP + 65 * 32768;
constexpr size_t OFF_X1 = OFF_V + 2097152;
constexpr size_t OFF_ST = OFF_X1 + 2097152;
constexpr size_t OFF_W1 = OFF_ST + 262144;
constexpr size_t OFF_W2 = OFF_W1 + 15 * 34 * 6144;
constexpr size_t OFF_H = OFF_W2 + 16 * 86 * 1024;

DI float bf2f(unsigned v) { return __builtin_bit_cast(float, v << 16); }
DI float bflo(unsigned v) { return __builtin_bit_cast(float, v << 16); }
DI float bfhi(unsigned v) { return __builtin_bit_cast(float, v & 0xffff0000u); }
DI unsigned pkbf(float a, float b) { f2 v = {a, b}; return __builtin_bit_cast(unsigned, __builtin_convertvector(v, b2)); }
DI unsigned pkh(float a, float b) { f2 v = {a, b}; return __builtin_bit_cast(unsigned, __builtin_convertvector(v, h2)); }
DI float rbf(float a) { return bflo(pkbf(a, a)); }
DI float cosr(float rev) { return __builtin_amdgcn_cosf(rev); }
DI float sinr(float rev) { return __builtin_amdgcn_sinf(rev); }
DI b2 asB2(unsigned v) { return __builtin_bit_cast(b2, v); }
DI h2 asH2(unsigned v) { return __builtin_bit_cast(h2, v); }
DI unsigned bfi(unsigned m, unsigned a, unsigned b) { return (m & a) | (~m & b); }
DI h8 asH(u4 v) { return __builtin_bit_cast(h8, v); }
DI b8 asB(u4 v) { return __builtin_bit_cast(b8, v); }
DI f4 mm16(h8 a, h8 b, f4 c) { return __builtin_amdgcn_mfma_f32_16x16x32_f16(a, b, c, 0, 0, 0); }
DI f16v mm32h(h8 a, h8 b, f16v c) { return __builtin_amdgcn_mfma_f32_32x32x16_f16(a, b, c, 0, 0, 0); }
DI f16v mm32b(b8 a, b8 b, f16v c) { return __builtin_amdgcn_mfma_f32_32x32x16_bf16(a, b, c, 0, 0, 0); }
DI h8 pack8(f4 r, f4 i) { u4 v = {pkh(r[0], r[1]), pkh(r[2], r[3]), pkh(i[0], i[1]), pkh(i[2], i[3])}; return asH(v); }
template <int CTRL> DI float dppadd(float v) { return v + __builtin_amdgcn_update_dpp(0.f, v, CTRL, 0xf, 0xf, false); }
DI float sum16(float v) { v = dppadd<0x128>(v); v = dppadd<0x124>(v); v = dppadd<0x122>(v); return dppadd<0x121>(v); }
DI float wavemax(float v) {
  for (int o = 32; o; o >>= 1) v = fmaxf(v, __shfl_xor(v, o));
  return v;
}
DI unsigned scale_exp(float m) { unsigned e = (__builtin_bit_cast(unsigned, m) >> 23) & 0xff; return min(max(e, 1u), 252u); }
DI float scale_up(unsigned e) { return __builtin_bit_cast(float, (e + 1) << 23); }
DI float scale_inv(unsigned e) { return __builtin_bit_cast(float, (253 - e) << 23); }
DI float mod_alpha_max(const float* mmax) {
  float m = 0.f;
  for (int i = 0; i < 16; ++i) m = fmaxf(m, mmax[i]);
  return m;
}

constexpr int KA_MAIN = 256, KA_GEMV = 48, KA_WPREP = 65, KA_W2 = 86;

DI void kA_main(const u16* __restrict__ x, char* ws, char* smem, int b, int w, int l) {
  const int s2 = (b & 7) * 2 + ((b >> 3) & 1), c = b >> 4;
  const int rsub = l >> 4, j = l & 15;
  u4 xv[4][4];
#pragma unroll
  for (int g = 0; g < 4; ++g)
#pragma unroll
    for (int q = 0; q < 4; ++q)
      xv[g][q] = *(const u4*)(x + (size_t)(16 * (16 * w + 4 * g + rsub) + s2) * 512 + 128 * q + 8 * j);

  const int m = 16 * w + j, slot = m >> 1;
  const int keff = (m == 1) ? 64 : slot;
  const float ph = ((m & 1) && slot) ? 0.25f : 0.f;
  h8 bfr[4];
#pragma unroll
  for (int ks = 0; ks < 4; ++ks) {
    float v[8];
#pragma unroll
    for (int jj = 0; jj < 8; ++jj) {
      const int s1 = 32 * ks + 8 * rsub + jj;
      v[jj] = 0.08838834764831845f * cosr((float)((s1 * keff) & 127) * (1.f / 128.f) + ph);
    }
    u4 p = {pkh(v[0], v[1]), pkh(v[2], v[3]), pkh(v[4], v[5]), pkh(v[6], v[7])};
    bfr[ks] = asH(p);
  }

  _Float16* nT = (_Float16*)smem;
  const bool own = (j & 3) == (c >> 2);
  unsigned m1 = (c & 1) ? ~0u : 0u, m2 = (c & 2) ? ~0u : 0u;
  asm volatile("" : "+s"(m1), "+s"(m2), "+v"(bfr[0]), "+v"(bfr[1]), "+v"(bfr[2]), "+v"(bfr[3]));
  __builtin_amdgcn_sched_barrier(0);
  const b2 ones = {(__bf16)1.0f, (__bf16)1.0f};
#pragma unroll
  for (int g = 0; g < 4; ++g) {
    float sm = 0.f, sq = 0.f;
    unsigned sel[4];
#pragma unroll
    for (int q = 0; q < 4; ++q) {
#pragma unroll
      for (int e = 0; e < 4; ++e) {
        const b2 p = asB2(xv[g][q][e]);
        sm = __builtin_amdgcn_fdot2_f32_bf16(p, ones, sm, false);
        sq = __builtin_amdgcn_fdot2_f32_bf16(p, p, sq, false);
      }
      sel[q] = bfi(m2, bfi(m1, xv[g][q][3], xv[g][q][2]), bfi(m1, xv[g][q][1], xv[g][q][0]));
    }
    sm = sum16(sm);
    sq = sum16(sq);
    const float mean = sm * (1.f / 512.f);
    const float var = fmaxf(sq * (1.f / 512.f) - mean * mean, 0.f);
    const float mb = rbf(mean);
    const float sd = rbf(__builtin_amdgcn_sqrtf(rbf(rbf(var) + 1e-5f)));
    const float rs = __builtin_amdgcn_rcpf(sd);
    const int s1 = 16 * w + 4 * g + rsub;
#pragma unroll
    for (int q = 0; q < 4; ++q) {
      const unsigned t = pkbf(bflo(sel[q]) - mb, bfhi(sel[q]) - mb);
      const unsigned n = pkbf(bflo(t) * rs, bfhi(t) * rs);
      const int d1 = 4 * q + (j >> 2);
      if (own) {
        nT[d1 * 136 + s1] = (_Float16)bflo(n);
        nT[(16 + d1) * 136 + s1] = (_Float16)bfhi(n);
      }
    }
  }
  __syncthreads();
  char* yb = ws + OFF_Y + (size_t)slot * 32768 + s2 * 2048 + (2 * c) * 64 + (m & 1) * 32 + rsub * 8;
#pragma unroll
  for (int t = 0; t < 2; ++t) {
    f4 acc = {0.f, 0.f, 0.f, 0.f};
#pragma unroll
    for (int ks = 0; ks < 4; ++ks) {
      const h8 a = *(const h8*)(nT + (16 * t + j) * 136 + 32 * ks + 8 * rsub);
      acc = mm16(a, bfr[ks], acc);
    }
    u2 o = {pkh(acc[0], acc[1]), pkh(acc[2], acc[3])};
    *(u2*)(yb + t * 64) = o;
  }
}

DI void kA_gemv(const u16* __restrict__ cvec, const u16* __restrict__ wmod, char* ws, char* smem, int bb, int tid) {
  const int oct = tid & 7, rg = tid >> 3, l = tid & 63, w = tid >> 6;
  u4 wv[8];
  u16 cv[8];
#pragma unroll
  for (int i = 0; i < 8; ++i) {
    wv[i] = *(const u4*)(wmod + (size_t)(rg + 64 * i) * 3072 + 64 * bb + 8 * oct);
    cv[i] = cvec[rg + 64 * i];
  }
  float acc[8];
#pragma unroll
  for (int e = 0; e < 8; ++e) acc[e] = 0.f;
#pragma unroll
  for (int i = 0; i < 8; ++i) {
    const float cf = bf2f(cv[i]);
    const float sl = rbf(cf / (1.f + __expf(-cf)));
#pragma unroll
    for (int e = 0; e < 4; ++e) {
      acc[2 * e] = fmaf(sl, bflo(wv[i][e]), acc[2 * e]);
      acc[2 * e + 1] = fmaf(sl, bfhi(wv[i][e]), acc[2 * e + 1]);
    }
  }
  float* red = (float*)smem;
#pragma unroll
  for (int e = 0; e < 8; ++e) {
    float v = acc[e];
    v += __shfl_xor(v, 8);
    v += __shfl_xor(v, 16);
    v += __shfl_xor(v, 32);
    if (l < 8) red[w * 64 + 8 * l + e] = v;
  }
  __syncthreads();
  if (tid < 64) {
    float v = 0.f;
#pragma unroll
    for (int i = 0; i < 8; ++i) v += red[i * 64 + tid];
    v = rbf(v);
    const int sec = bb >> 3;
    if (sec == 1 || sec == 4) v = rbf(1.f + v);
    ((float*)(ws + OFF_MOD))[64 * bb + tid] = v;
    const float mx = wavemax(fabsf(v));
    if (tid == 0) ((float*)(ws + OFF_MMAX))[bb] = mx;
  }
}

DI void kA_wprep(const u16* __restrict__ wr, const u16* __restrict__ wi, char* ws, char* smem, int k1, int tid) {
#pragma unroll
  for (int it = 0; it < 4; ++it) {
    const int i = tid + 512 * it, row = i >> 5, pc = i & 31;
    const int ksA = k1 + 128 * (row & 15);
    const int ks = (row >> 5) ? ((2048 - ksA) & 2047) : ksA;
    const char* src = (const char*)(((row >> 4) & 1) ? wi : wr) + ((ks * 514) & ~3) + pc * 16;
    *(u4*)(smem + row * 528 + pc * 16) = *(const u4a*)src;
  }
  if (tid < 64) {
    const int row = tid;
    const int ksA = k1 + 128 * (row & 15);
    const int ks = (row >> 5) ? ((2048 - ksA) & 2047) : ksA;
    const char* src = (const char*)(((row >> 4) & 1) ? wi : wr) + ((ks * 514) & ~3) + 512;
    *(unsigned*)(smem + row * 528 + 512) = *(const unsigned*)src;
  }
  __syncthreads();
  const int mis = (k1 & 1) * 2;
  const int l = tid & 63, k2 = l & 15, q = l >> 4;
  float re[4][4], im[4][4], mx = 0.f;
#pragma unroll
  for (int it = 0; it < 4; ++it) {
    const int i = tid + 512 * it, hh = (i >> 6) & 1, j1 = i >> 7;
#pragma unroll
    for (int r = 0; r < 4; ++r) {
      const int kd = j1 + 16 * (16 * hh + 4 * q + r);
      const int idx = hh ? 512 - kd : kd;
      const char* base = smem + (hh * 32 + k2) * 528 + mis + idx * 2;
      float a = bf2f(*(const u16*)base);
      float bi = bf2f(*(const u16*)(base + 16 * 528));
      if (hh) bi = -bi;
      if ((kd & 255) == 0) {
        const char* ob = smem + ((hh ^ 1) * 32 + k2) * 528 + mis + idx * 2;
        const float a2 = bf2f(*(const u16*)ob);
        float b2v = bf2f(*(const u16*)(ob + 16 * 528));
        if (!hh) b2v = -b2v;
        a = 0.5f * (a + a2);
        bi = 0.5f * (bi + b2v);
      }
      re[it][r] = a;
      im[it][r] = bi;
      mx = fmaxf(mx, fmaxf(fabsf(a), fabsf(bi)));
    }
  }
  mx = wavemax(mx);
  float* red = (float*)(smem + 64 * 528);
  if (l == 0) red[tid >> 6] = mx;
  __syncthreads();
#pragma unroll
  for (int i = 0; i < 8; ++i) mx = fmaxf(mx, red[i]);
  const unsigned e = scale_exp(mx);
  const float inv = scale_inv(e);
  if (tid == 0) ((float*)(ws + OFF_BETA))[k1] = scale_up(e);
#pragma unroll
  for (int it = 0; it < 4; ++it) {
    u4 o;
#pragma unroll
    for (int r = 0; r < 4; ++r) o[r] = pkh(re[it][r] * inv, im[it][r] * inv);
    *(u4*)(ws + OFF_WP + (size_t)k1 * 32768 + (tid + 512 * it) * 16) = o;
  }
}

DI s4 trread(const char* p) { return __builtin_amdgcn_ds_read_tr16_b64_v4i16((LDS3 s4*)p); }

DI void kA_w2(const u16* __restrict__ wd, char* ws, char* smem, int ks, int tid) {
#pragma unroll
  for (int it = 0; it < 2; ++it) {
    const int i = tid + 512 * it, row = i >> 6, pc = i & 63;
    *(u4*)(smem + row * 1088 + pc * 16) = *(const u4*)(wd + (size_t)(16 * ks + row) * 512 + pc * 8);
  }
  __syncthreads();
  const int l = tid & 63, w = tid >> 6, h = l >> 5, gi = (l >> 4) & 1, q = (l & 15) >> 2, p = l & 3;
#pragma unroll
  for (int it = 0; it < 2; ++it) {
    const int nt = w + 8 * it;
    const char* a = smem + (8 * h + q) * 1088 + (32 * nt + 16 * gi + 4 * p) * 2;
    const s4 lo = trread(a), hi = trread(a + 4 * 1088);
    const u2 l2 = __builtin_bit_cast(u2, lo), h2v = __builtin_bit_cast(u2, hi);
    u4 o = {l2[0], l2[1], h2v[0], h2v[1]};
    *(u4*)(ws + OFF_W2 + ((size_t)nt * 86 + ks) * 1024 + l * 16) = o;
  }
}

__global__ __launch_bounds__(512) void kA(const u16* __restrict__ x, const u16* __restrict__ wr,
                                          const u16* __restrict__ wi, const u16* __restrict__ wd,
                                          const u16* __restrict__ cvec, const u16* __restrict__ wmod, char* ws) {
  __shared__ __attribute__((aligned(16))) char smem[64 * 528 + 64];
  const int b = blockIdx.x, tid = threadIdx.x;
  if (b < KA_MAIN) kA_main(x, ws, smem, b, __builtin_amdgcn_readfirstlane(tid >> 6), tid & 63);
  else if (b < KA_MAIN + KA_GEMV) kA_gemv(cvec, wmod, ws, smem, b - KA_MAIN, tid);
  else if (b < KA_MAIN + KA_GEMV + KA_WPREP) kA_wprep(wr, wi, ws, smem, b - KA_MAIN - KA_GEMV, tid);
  else kA_w2(wd, ws, smem, b - KA_MAIN - KA_GEMV - KA_WPREP, tid);
}

constexpr int KB_MAIN = 65, KB_W1 = 176;
constexpr int T1_BYTES = 16 * 2064, T2_BYTES = 32768;

DI void cs4(float r0, float step, float sc, unsigned& c01, unsigned& c23, unsigned& s01, unsigned& s23) {
  float c[4], s[4];
#pragma unroll
  for (int i = 0; i < 4; ++i) {
    const float r = __builtin_amdgcn_fractf(r0 + step * i);
    c[i] = sc * cosr(r);
    s[i] = sc * sinr(r);
  }
  c01 = pkh(c[0], c[1]); c23 = pkh(c[2], c[3]); s01 = pkh(s[0], s[1]); s23 = pkh(s[2], s[3]);
}
DI h8 frag4(unsigned a, unsigned b, unsigned c, unsigned d) { u4 v = {a, b, c, d}; return asH(v); }
constexpr unsigned NEG2 = 0x80008000u;

DI void kB_main(char* ws, char* smem, int k1, int w, int l) {
  const int i16 = l & 15, g = l >> 4;
  const int slot = k1 & 63;
  const bool realonly = (slot == 0);
  const float* mod = (const float*)(ws + OFF_MOD);
  u4 yv[4], wv[2][2];
  {
    const int go = realonly ? ((g & 1) + (k1 >> 6) * 2) : g;
    const char* yb = ws + OFF_Y + (size_t)slot * 32768 + i16 * 2048 + (4 * w) * 64 + go * 16;
#pragma unroll
    for (int t = 0; t < 4; ++t) yv[t] = *(const u4*)(yb + t * 64);
  }
  f4 c1[8], sa[8];
#pragma unroll
  for (int jj = 0; jj < 8; ++jj) c1[jj] = *(const f4*)(mod + 512 + 32 * (8 * (g & 1) + jj) + 4 * w);
  if (k1 == 0) {
#pragma unroll
    for (int jj = 0; jj < 8; ++jj) sa[jj] = *(const f4*)(mod + 32 * (8 * (g & 1) + jj) + 4 * w);
  } else {
#pragma unroll
    for (int jj = 0; jj < 8; ++jj) sa[jj] = f4{0.f, 0.f, 0.f, 0.f};
  }
#pragma unroll
  for (int t = 0; t < 2; ++t)
#pragma unroll
    for (int hh = 0; hh < 2; ++hh)
      wv[t][hh] = *(const u4*)(ws + OFF_WP + (size_t)k1 * 32768 + (((2 * w + t) * 2 + hh) * 64 + l) * 16);
  const float ia = scale_inv(scale_exp(mod_alpha_max((const float*)(ws + OFF_MMAX))));

  h8 f16re, f16im;
  {
    unsigned a0, a1, a2, a3, b0, b1, b2_, b3;
    const float st = (float)i16 * (1.f / 16.f), r0 = st * (8 * (g & 1));
    cs4(r0, st, 0.25f, a0, a1, b0, b1);
    cs4(r0 + 4.f * st, st, 0.25f, a2, a3, b2_, b3);
    if (g < 2) { f16re = frag4(a0, a1, a2, a3); f16im = frag4(b0 ^ NEG2, b1 ^ NEG2, b2_ ^ NEG2, b3 ^ NEG2); }
    else       { f16re = frag4(b0, b1, b2_, b3); f16im = frag4(a0, a1, a2, a3); }
  }
  h8 gre, gim, ghre, ghim;
  {
    unsigned c01, c23, s01, s23;
    const float st = (float)(k1 + 128 * i16) * (1.f / 2048.f);
    cs4(st * (4 * g), st, 0.25f, c01, c23, s01, s23);
    gre = frag4(c01, c23, s01, s23);
    gim = frag4(s01 ^ NEG2, s23 ^ NEG2, c01, c23);
    cs4((float)(i16 * (k1 + 512 * g)) * (1.f / 2048.f), (float)i16 * (1.f / 16.f), 0.25f, c01, c23, s01, s23);
    ghre = frag4(c01, c23, s01 ^ NEG2, s23 ^ NEG2);
    ghim = frag4(s01, s23, c01, c23);
  }
  unsigned C32[2][2][2], S32[2][2][2];
#pragma unroll
  for (int a = 0; a < 2; ++a)
#pragma unroll
    for (int bq = 0; bq < 2; ++bq) {
      const float st = (float)(16 * a + i16) * (1.f / 32.f);
      cs4(st * (16 * bq + 4 * g), st, 0.17677669529663687f, C32[a][bq][0], C32[a][bq][1], S32[a][bq][0], S32[a][bq][1]);
    }

  const float keep = (realonly && g >= 2) ? 0.f : ia;
  const float sak = (g >= 2) ? 0.f : ia * 11.313708498984761f;
  float pre[4][4], pim[4][4];
#pragma unroll
  for (int t = 0; t < 4; ++t) {
    u4 mv, av;
#pragma unroll
    for (int e = 0; e < 4; ++e) {
      mv[e] = pkh(c1[2 * e][t] * keep, c1[2 * e + 1][t] * keep);
      av[e] = pkh(sa[2 * e][t] * sak, sa[2 * e + 1][t] * sak);
    }
    const h8 a = __builtin_elementwise_fma(asH(yv[t]), asH(mv), asH(av));
    const f4 z = {0.f, 0.f, 0.f, 0.f};
    const f4 dr = mm16(a, f16re, z), di = mm16(a, f16im, z);
    const h8 xb = pack8(dr, di);
    const f4 pr = mm16(gre, xb, z), pi = mm16(gim, xb, z);
    const float r = (float)((4 * w + t) * i16) * (1.f / 512.f);
    const float tc = cosr(r), ts = sinr(r);
#pragma unroll
    for (int e = 0; e < 4; ++e) {
      pre[t][e] = pr[e] * tc + pi[e] * ts;
      pim[t][e] = pi[e] * tc - pr[e] * ts;
    }
  }
  char* T1 = smem;
  char* T2 = smem + T1_BYTES;
#pragma unroll
  for (int e = 0; e < 4; ++e) {
    const int k2 = 4 * g + e;
    u4 o = {pkh(pre[0][e], pre[1][e]), pkh(pre[2][e], pre[3][e]), pkh(pim[0][e], pim[1][e]), pkh(pim[2][e], pim[3][e])};
    *(u4*)(T1 + i16 * 2064 + k2 * 128 + ((w ^ ((k2 >> 1) & 7)) * 16)) = o;
  }
  __syncthreads();

  float qre[2][2][4], qim[2][2][4];
#pragma unroll
  for (int t = 0; t < 2; ++t) {
    const int j1 = 2 * w + t;
    const char* tb = T1 + j1 * 2064 + i16 * 128;
    const h8 b0 = *(const h8*)(tb + ((g ^ ((i16 >> 1) & 7)) * 16));
    const h8 b1 = *(const h8*)(tb + (((4 + g) ^ ((i16 >> 1) & 7)) * 16));
    const f4 z = {0.f, 0.f, 0.f, 0.f};
    h8 za[2];
#pragma unroll
    for (int hh = 0; hh < 2; ++hh) {
      f4 xr = mm16(frag4(C32[hh][0][0], C32[hh][0][1], S32[hh][0][0], S32[hh][0][1]), b0, z);
      xr = mm16(frag4(C32[hh][1][0], C32[hh][1][1], S32[hh][1][0], S32[hh][1][1]), b1, xr);
      f4 xi = mm16(frag4(S32[hh][0][0] ^ NEG2, S32[hh][0][1] ^ NEG2, C32[hh][0][0], C32[hh][0][1]), b0, z);
      xi = mm16(frag4(S32[hh][1][0] ^ NEG2, S32[hh][1][1] ^ NEG2, C32[hh][1][0], C32[hh][1][1]), b1, xi);
      f4 zr, zi;
#pragma unroll
      for (int e = 0; e < 4; ++e) {
        const h2 wq = asH2(wv[t][hh][e]);
        const float wre = (float)wq[0], wim = (float)wq[1];
        zr[e] = xr[e] * wre - xi[e] * wim;
        zi[e] = xr[e] * wim + xi[e] * wre;
      }
      za[hh] = pack8(zr, zi);
    }
#pragma unroll
    for (int dh = 0; dh < 2; ++dh) {
      f4 ur = mm16(za[0], frag4(C32[dh][0][0], C32[dh][0][1], S32[dh][0][0] ^ NEG2, S32[dh][0][1] ^ NEG2), z);
      ur = mm16(za[1], frag4(C32[dh][1][0], C32[dh][1][1], S32[dh][1][0] ^ NEG2, S32[dh][1][1] ^ NEG2), ur);
      f4 ui = mm16(za[0], frag4(S32[dh][0][0], S32[dh][0][1], C32[dh][0][0], C32[dh][0][1]), z);
      ui = mm16(za[1], frag4(S32[dh][1][0], S32[dh][1][1], C32[dh][1][0], C32[dh][1][1]), ui);
      const h8 ub = pack8(ur, ui);
      const f4 vr = mm16(ghre, ub, z), vi = mm16(ghim, ub, z);
      const float r = (float)((16 * dh + i16) * j1) * (1.f / 512.f);
      const float tc = cosr(r), ts = sinr(r);
#pragma unroll
      for (int e = 0; e < 4; ++e) {
        qre[t][dh][e] = vr[e] * tc - vi[e] * ts;
        qim[t][dh][e] = vi[e] * tc + vr[e] * ts;
      }
    }
  }
#pragma unroll
  for (int dh = 0; dh < 2; ++dh)
#pragma unroll
    for (int e = 0; e < 4; ++e) {
      const int s2 = 4 * g + e, d2 = 16 * dh + i16;
      u2 o = {pkh(qre[0][dh][e], qre[1][dh][e]), pkh(qim[0][dh][e], qim[1][dh][e])};
      *(u2*)(T2 + (s2 * 32 + d2) * 64 + (((w >> 1) ^ ((d2 >> 2) & 3)) * 16) + (w & 1) * 8) = o;
    }
  const int n32 = l & 31, h = l >> 5, ro = n32 >> 4, d1 = n32 & 15;
  h8 if16[2];
#pragma unroll
  for (int st = 0; st < 2; ++st) {
    float v[8];
#pragma unroll
    for (int jj = 0; jj < 8; ++jj) {
      const int j1 = 4 * (2 * st + h) + 2 * (jj >> 2) + (jj & 1), ri = (jj >> 1) & 1;
      v[jj] = 0.25f * cosr((float)((d1 * j1) & 15) * (1.f / 16.f) + (float)(ri - ro) * 0.25f);
    }
    if16[st] = frag4(pkh(v[0], v[1]), pkh(v[2], v[3]), pkh(v[4], v[5]), pkh(v[6], v[7]));
  }
  __syncthreads();

  const int krow = realonly ? (k1 >> 6) : (2 * slot + ro);
  const int swz = (((krow >> 3) & 1) * 8) ^ (((krow >> 1) & 1) * 64);
#pragma unroll
  for (int t = 0; t < 2; ++t) {
    const int s2 = 2 * w + t;
    const char* tb = T2 + (s2 * 32 + n32) * 64;
    f16v acc;
#pragma unroll
    for (int e = 0; e < 16; ++e) acc[e] = 0.f;
#pragma unroll
    for (int st = 0; st < 2; ++st) {
      const h8 a = *(const h8*)(tb + (((2 * st + h) ^ ((n32 >> 2) & 3)) * 16));
      acc = mm32h(a, if16[st], acc);
    }
    if (!realonly || ro == 0) {
      char* vb = ws + OFF_V + ((size_t)(s2 * 8 + (d1 >> 1)) * 128 + krow) * 128;
#pragma unroll
      for (int qd = 0; qd < 4; ++qd) {
        u2 o = {pkh(acc[4 * qd], acc[4 * qd + 1]), pkh(acc[4 * qd + 2], acc[4 * qd + 3])};
        *(u2*)(vb + ((((d1 & 1) * 32 + 8 * qd + 4 * h) * 2) ^ swz)) = o;
      }
    }
  }
}

DI void kB_w1(const u16* __restrict__ wg, const u16* __restrict__ wu, char* ws, char* smem, int bb, int tid) {
  const int cg = bb >> 3, db = bb & 7;
  constexpr int RS = 136, MO = 64 * RS + 32;
  const float* mod = (const float*)(ws + OFF_MOD);
  {
    const int row = tid >> 3, oct = tid & 7;
    const int col = min(64 * cg + 8 * oct, FH - 8);
    const size_t o = (size_t)(64 * db + row) * FH + col;
    const u4 a = *(const u4*)(wg + o), bq = *(const u4*)(wu + o);
    char* p = smem + row * RS + oct * 16;
    *(u2*)p = u2{a[0], a[1]};
    *(u2*)(p + 8) = u2{a[2], a[3]};
    *(u2*)(p + MO) = u2{bq[0], bq[1]};
    *(u2*)(p + MO + 8) = u2{bq[2], bq[3]};
  }
  const int l = tid & 63, w = tid >> 6, r = l & 31, h = l >> 5;
  const int ft = w >> 1, kp = w & 1;
  f4 c1[2][2], sb[2][2];
#pragma unroll
  for (int it = 0; it < 2; ++it) {
    const int d0 = 64 * db + 16 * (2 * kp + it) + 8 * h;
    c1[it][0] = *(const f4*)(mod + 2048 + d0);
    c1[it][1] = *(const f4*)(mod + 2048 + d0 + 4);
    sb[it][0] = *(const f4*)(mod + 1536 + d0);
    sb[it][1] = *(const f4*)(mod + 1536 + d0 + 4);
  }
  __syncthreads();
  const int fl = 8 * ((r >> 2) & 1) + 2 * (r >> 3) + ((r >> 1) & 1);
  const int T = 4 * cg + ft;
  const int chunk = T / 6, nt = T - 6 * chunk;
  char* wb = ws + OFF_W1 + (size_t)chunk * 34 * 6144 + nt * 1024;
  float P = 0.f, Q = 0.f;
#pragma unroll
  for (int it = 0; it < 2; ++it) {
    const int ksl = 2 * kp + it;
    const char* p = smem + (r & 1) * MO + (16 * ksl + 8 * h) * RS + (16 * ft + fl) * 2;
    u4 o;
#pragma unroll
    for (int e = 0; e < 4; ++e) {
      const float v0 = bf2f(*(const u16*)(p + (2 * e) * RS)), v1 = bf2f(*(const u16*)(p + (2 * e + 1) * RS));
      const float ca = c1[it][e >> 1][(2 * e) & 3], cb = c1[it][e >> 1][(2 * e + 1) & 3];
      o[e] = pkbf(v0 * ca, v1 * cb);
      P += bflo(o[e]) + bfhi(o[e]);
      Q = fmaf(v0, sb[it][e >> 1][(2 * e) & 3], Q);
      Q = fmaf(v1, sb[it][e >> 1][(2 * e + 1) & 3], Q);
    }
    if (T < 86) *(u4*)(wb + (size_t)(4 * db + ksl) * 6144 + l * 16) = o;
  }
  P += __shfl_xor(P, 32);
  Q += __shfl_xor(Q, 32);
  float* red = (float*)(smem + 2 * MO);
  if (l < 32) *(f2*)(red + (w * 32 + r) * 2) = f2{P, Q};
  __syncthreads();
  if (tid < 256) {
    const int pq = tid >> 7, ft2 = (tid >> 5) & 3, r2 = tid & 31;
    const float v = red[((2 * ft2) * 32 + r2) * 2 + pq] + red[((2 * ft2 + 1) * 32 + r2) * 2 + pq];
    const float hi = rbf(v);
    const int T2i = 4 * cg + ft2, ch2 = T2i / 6, nt2 = T2i - 6 * ch2;
    if (T2i < 86)
      *(unsigned*)(ws + OFF_W1 + (size_t)ch2 * 34 * 6144 + (32 + pq) * 6144 + nt2 * 1024 + (r2 + 32 * (db >> 2)) * 16 + (db & 3) * 4) =
          pkbf(hi, v - hi);
  }
}

__global__ __launch_bounds__(512) void kB(const u16* __restrict__ wg, const u16* __restrict__ wu, char* ws) {
  __shared__ __attribute__((aligned(16))) char smem[T1_BYTES + T2_BYTES];
  const int b = blockIdx.x, tid = threadIdx.x;
  if (b < KB_MAIN) kB_main(ws, smem, b, __builtin_amdgcn_readfirstlane(tid >> 6), tid & 63);
  else kB_w1(wg, wu, ws, smem, b - KB_MAIN, tid);
}

__global__ __launch_bounds__(512) void kC(const u16* __restrict__ x, char* ws) {
  __shared__ __attribute__((aligned(16))) char smem[16384];
  const int b = blockIdx.x, tid = threadIdx.x;
  const int w = __builtin_amdgcn_readfirstlane(tid >> 6), l = tid & 63;
  const int s2 = (b & 7) * 2 + ((b >> 3) & 1), hs = (b >> 4) & 1, c8 = b >> 5;
  const int i16 = l & 15, g = l >> 4, st = w & 3, dh = w >> 2;
  const char* vsrc = ws + OFF_V + (size_t)(s2 * 8 + c8) * 16384 + tid * 16;
  const u4 v0 = *(const u4*)vsrc, v1 = *(const u4*)(vsrc + 8192);
  const int s1 = 64 * hs + 16 * st + i16;
  const int d0 = 64 * c8 + 32 * dh + 8 * g;
  const u4 xv = *(const u4*)(x + (size_t)(16 * s1 + s2) * 512 + d0);
  const float* mod = (const float*)(ws + OFF_MOD);
  const f4 ga0 = *(const f4*)(mod + 1024 + d0), ga1 = *(const f4*)(mod + 1024 + d0 + 4);
  const float* beta = (const float*)(ws + OFF_BETA);
  f4 bt[4];
#pragma unroll
  for (int ks = 0; ks < 4; ++ks) bt[ks] = *(const f4*)(beta + 16 * ks + 4 * g);
  const float b64 = beta[64];
  const float bmax = wavemax(fmaxf(beta[l], b64));
  const float alpha = scale_up(scale_exp(mod_alpha_max((const float*)(ws + OFF_MMAX))));
  const float ib = 0.08838834764831845f / bmax;

  h8 bfr[4];
#pragma unroll
  for (int ks = 0; ks < 4; ++ks) {
    unsigned p[4];
#pragma unroll
    for (int e = 0; e < 4; ++e) {
      const int slot = 16 * ks + 4 * g + e;
      const float r = (float)((s1 * slot) & 127) * (1.f / 128.f);
      float cv = 2.f * ib * bt[ks][e] * cosr(r), sv = -2.f * ib * bt[ks][e] * sinr(r);
      if (ks == 0 && e == 0 && g == 0) { cv = ib * bt[0][0]; sv = (s1 & 1) ? -ib * b64 : ib * b64; }
      p[e] = pkh(cv, sv);
    }
    bfr[ks] = frag4(p[0], p[1], p[2], p[3]);
  }
  *(u4*)(smem + tid * 16) = v0;
  *(u4*)(smem + 8192 + tid * 16) = v1;
  __syncthreads();

  const int q = i16 >> 2, p = i16 & 3;
  f4 acc[2];
#pragma unroll
  for (int t = 0; t < 2; ++t) {
    acc[t] = f4{0.f, 0.f, 0.f, 0.f};
#pragma unroll
    for (int ks = 0; ks < 4; ++ks) {
      const int k0 = 32 * ks + 8 * g + q;
      const int cb = ((32 * dh + 8 * p + 4 * t) * 2) ^ ((g & 1) * 8) ^ (((q >> 1) & 1) * 64);
      const u2 lo = __builtin_bit_cast(u2, trread(smem + k0 * 128 + cb));
      const u2 hi = __builtin_bit_cast(u2, trread(smem + (k0 + 4) * 128 + cb));
      acc[t] = mm16(frag4(lo[0], lo[1], hi[0], hi[1]), bfr[ks], acc[t]);
    }
  }
  const float ysc = bmax * alpha;
  u4 o;
  float sm = 0.f, sq = 0.f;
#pragma unroll
  for (int t = 0; t < 2; ++t)
#pragma unroll
    for (int e = 0; e < 2; ++e) {
      const f4 gv = t ? ga1 : ga0;
      const unsigned yb = pkbf(acc[t][2 * e] * ysc, acc[t][2 * e + 1] * ysc);
      const unsigned gy = pkbf(bflo(yb) * gv[2 * e], bfhi(yb) * gv[2 * e + 1]);
      const unsigned xx = xv[2 * t + e];
      const unsigned r = pkbf(bflo(xx) + bflo(gy), bfhi(xx) + bfhi(gy));
      o[2 * t + e] = r;
      const float a = bflo(r), bq = bfhi(r);
      sm += a + bq;
      sq = fmaf(a, a, fmaf(bq, bq, sq));
    }
  *(u4*)(ws + OFF_X1 + ((size_t)(s2 * 4 + (s1 >> 5)) * 32 + (d0 >> 4)) * 1024 + ((s1 & 31) + 32 * (g & 1)) * 16) = o;
  sm += __shfl_xor(sm, 16); sq += __shfl_xor(sq, 16);
  sm += __shfl_xor(sm, 32); sq += __shfl_xor(sq, 32);
  if (g == 0) *(f2*)(ws + OFF_ST + (size_t)(s2 * 128 + s1) * 128 + (2 * c8 + dh) * 8) = f2{sm, sq};
}

constexpr int G1_SLOT = 24576, G1_NSLOT = 6, G1_EXTRA = G1_SLOT * G1_NSLOT, G1_LDS = G1_EXTRA + 12288;

DI void ld4(u4& a, u4& b, u4& c, u4& d, unsigned voff, const char* base) {
  asm volatile("s_nop 4\n\tglobal_load_dwordx4 %0, %4, %5\n\tglobal_load_dwordx4 %1, %4, %5 offset:1024\n\t"
               "global_load_dwordx4 %2, %4, %5 offset:2048\n\tglobal_load_dwordx4 %3, %4, %5 offset:3072"
               : "=&v"(a), "=&v"(b), "=&v"(c), "=&v"(d) : "v"(voff), "s"(base) : "memory");
}
template <int N> DI void glds(unsigned voff, const char* base, unsigned lds) {
  unsigned keep;
  if constexpr (N == 3)
    asm volatile("s_mov_b32 %0, m0\n\ts_mov_b32 m0, %3\n\ts_nop 4\n\tglobal_load_lds_dwordx4 %1, %2\n\t"
                 "global_load_lds_dwordx4 %1, %2 offset:1024\n\tglobal_load_lds_dwordx4 %1, %2 offset:2048\n\ts_mov_b32 m0, %0"
                 : "=&s"(keep) : "v"(voff), "s"(base), "s"(lds) : "memory");
  else
    asm volatile("s_mov_b32 %0, m0\n\ts_mov_b32 m0, %3\n\ts_nop 4\n\tglobal_load_lds_dwordx4 %1, %2\n\ts_mov_b32 m0, %0"
                 : "=&s"(keep) : "v"(voff), "s"(base), "s"(lds) : "memory");
}
template <int N> DI void wait4(u4& a, u4& b, u4& c, u4& d) {
  asm volatile("s_waitcnt vmcnt(%4)" : "+v"(a), "+v"(b), "+v"(c), "+v"(d) : "i"(N) : "memory");
}

template <int NT> DI void g1_body(char* ws, char* smem, int s2, int chunk, int w, int l) {
  constexpr int HN = NT / 2, PER = 4 + NT;
  const unsigned voff = l * 16;
  const unsigned lds0 = (unsigned)(size_t)(LDS3 char*)smem;
  const char* abase = ws + OFF_X1 + (size_t)(s2 * 4 + w) * 32768;
  const char* wbase = ws + OFF_W1 + (size_t)chunk * 34 * 6144;
  u4 stv[4], act[32];
  {
    const char* sb = ws + OFF_ST + (size_t)(s2 * 128 + 32 * w) * 128;
    const unsigned so = (l & 31) * 128 + (l >> 5) * 64;
    asm volatile("s_nop 4\n\tglobal_load_dwordx4 %0, %4, %5\n\tglobal_load_dwordx4 %1, %4, %5 offset:16\n\t"
                 "global_load_dwordx4 %2, %4, %5 offset:32\n\tglobal_load_dwordx4 %3, %4, %5 offset:48"
                 : "=&v"(stv[0]), "=&v"(stv[1]), "=&v"(stv[2]), "=&v"(stv[3]) : "v"(so), "s"(sb) : "memory");
  }
  auto issue = [&](int sidx, int slot) {
    ld4(act[4 * sidx], act[4 * sidx + 1], act[4 * sidx + 2], act[4 * sidx + 3], voff, abase + sidx * 4096);
    const char* src = wbase + (4 * sidx + w) * 6144;
    const unsigned dst = lds0 + slot * G1_SLOT + w * 6144;
    glds<HN>(voff, src, dst);
    glds<HN>(voff, src + HN * 1024, dst + HN * 1024);
  };
  issue(0, 0);
  glds<HN>(voff, wbase + (32 + (w >> 1)) * 6144 + (w & 1) * HN * 1024, lds0 + G1_EXTRA + (w >> 1) * 6144 + (w & 1) * HN * 1024);
#pragma unroll
  for (int s = 1; s < G1_NSLOT; ++s) issue(s, s);

  f16v acc[NT];
#pragma unroll
  for (int n = 0; n < NT; ++n)
#pragma unroll
    for (int e = 0; e < 16; ++e) acc[n][e] = 0.f;
  float mb = 0.f, sd = 0.f;

#pragma unroll
  for (int s = 0; s < 8; ++s) {
    if (s == 0) wait4<5 * PER + HN>(act[0], act[1], act[2], act[3]);
    else if (s <= 3) wait4<4 * PER>(act[4 * s], act[4 * s + 1], act[4 * s + 2], act[4 * s + 3]);
    else if (s == 4) wait4<3 * PER>(act[16], act[17], act[18], act[19]);
    else if (s == 5) wait4<2 * PER>(act[20], act[21], act[22], act[23]);
    else if (s == 6) wait4<PER>(act[24], act[25], act[26], act[27]);
    else wait4<0>(act[28], act[29], act[30], act[31]);
    __builtin_amdgcn_s_barrier();
    asm volatile("" ::: "memory");
    if (s == 1 || s == 2) issue(5 + s, s - 1);
    if (s == 1) {
      asm volatile("" : "+v"(stv[0]), "+v"(stv[1]), "+v"(stv[2]), "+v"(stv[3]));
      float sm = 0.f, sq = 0.f;
#pragma unroll
      for (int i = 0; i < 4; ++i) {
        const f4 v = __builtin_bit_cast(f4, stv[i]);
        sm += v[0] + v[2];
        sq += v[1] + v[3];
      }
      sm += __shfl_xor(sm, 32);
      sq += __shfl_xor(sq, 32);
      const float mean = sm * (1.f / 512.f);
      const float var = fmaxf(sq * (1.f / 512.f) - mean * mean, 0.f);
      mb = rbf(mean);
      sd = rbf(__builtin_amdgcn_sqrtf(rbf(rbf(var) + 1e-5f)));
    }
    const char* sl = smem + (s % G1_NSLOT) * G1_SLOT + l * 16;
#pragma unroll
    for (int k = 0; k < 4; ++k)
#pragma unroll
      for (int n = 0; n < NT; ++n) {
        const u4 a = *(const u4*)(sl + k * 6144 + n * 1024);
        acc[n] = mm32b(asB(a), asB(act[4 * s + k]), acc[n]);
      }
  }
  {
    const unsigned m2 = pkbf(-mb, -mb), d2 = pkbf(sd, sd);
    const u4 bm = {m2, m2, m2, m2}, bd = {d2, d2, d2, d2};
    const char* sl = smem + G1_EXTRA + l * 16;
#pragma unroll
    for (int n = 0; n < NT; ++n) {
      acc[n] = mm32b(asB(*(const u4*)(sl + n * 1024)), asB(bm), acc[n]);
      acc[n] = mm32b(asB(*(const u4*)(sl + 6144 + n * 1024)), asB(bd), acc[n]);
    }
  }
  const float rs = __builtin_amdgcn_rcpf(sd);
  char* hb = ws + OFF_H + ((size_t)(s2 * 4 + w) * 86 + chunk * 6) * 1024 + l * 16;
#pragma unroll
  for (int n = 0; n < NT; ++n) {
    u4 o;
#pragma unroll
    for (int e = 0; e < 4; ++e)
      o[e] = pkbf((acc[n][4 * e] * rs) * (acc[n][4 * e + 1] * rs), (acc[n][4 * e + 2] * rs) * (acc[n][4 * e + 3] * rs));
    *(u4*)(hb + n * 1024) = o;
  }
}

__global__ __launch_bounds__(256, 1) void kG1(char* ws) {
  extern __shared__ __attribute__((aligned(16))) char dsm[];
  const int b = blockIdx.x, tid = threadIdx.x;
  const int w = __builtin_amdgcn_readfirstlane(tid >> 6), l = tid & 63;
  const int s2 = (b & 7) * 2 + ((b >> 3) & 1), chunk = b >> 4;
  if (chunk < 14) g1_body<6>(ws, dsm, s2, chunk, w, l);
  else g1_body<2>(ws, dsm, s2, chunk, w, l);
}

constexpr int G2_RS = 68;

__global__ __launch_bounds__(256, 1) void kG2(char* ws, u16* __restrict__ out) {
  extern __shared__ __attribute__((aligned(16))) char dsm[];
  const int b = blockIdx.x, tid = threadIdx.x;
  const int w = __builtin_amdgcn_readfirstlane(tid >> 6), l = tid & 63;
  const int rt = (b & 7) * 4 + ((b >> 3) & 3), ct = b >> 5;
  const int k0 = (w == 3) ? 64 : 22 * w;
  const char* hp = ws + OFF_H + ((size_t)(2 * rt) * 86 + k0) * 1024 + l * 16;
  const char* wp = ws + OFF_W2 + ((size_t)(2 * ct) * 86 + k0) * 1024 + l * 16;
  f16v acc[2][2];
#pragma unroll
  for (int a = 0; a < 2; ++a)
#pragma unroll
    for (int c = 0; c < 2; ++c)
#pragma unroll
      for (int e = 0; e < 16; ++e) acc[a][c][e] = 0.f;
  u4 hf[22][2], wf[22][2];
#pragma unroll
  for (int k = 0; k < 22; ++k) {
    hf[k][0] = *(const u4*)(hp + k * 1024);
    wf[k][0] = *(const u4*)(wp + k * 1024);
    hf[k][1] = *(const u4*)(hp + (86 + k) * 1024);
    wf[k][1] = *(const u4*)(wp + (86 + k) * 1024);
  }
  const int row0 = tid >> 3, oct = tid & 7;
  const int dcol = 64 * ct + 8 * oct;
  u4 xr[2];
#pragma unroll
  for (int it = 0; it < 2; ++it)
    xr[it] = *(const u4*)(ws + OFF_X1 + ((size_t)(2 * rt + it) * 32 + (dcol >> 4)) * 1024 + (row0 + 32 * (oct & 1)) * 16);
  const float* mod = (const float*)(ws + OFF_MOD);
  const f4 gb0 = *(const f4*)(mod + 2560 + dcol), gb1 = *(const f4*)(mod + 2560 + dcol + 4);
#pragma unroll
  for (int k = 0; k < 22; ++k) {
    if (k < 2 && w == 3) continue;
#pragma unroll
    for (int a = 0; a < 2; ++a)
#pragma unroll
      for (int c = 0; c < 2; ++c) acc[a][c] = mm32b(asB(wf[k][c]), asB(hf[k][a]), acc[a][c]);
  }
  float* red = (float*)dsm;
#pragma unroll
  for (int a = 0; a < 2; ++a)
#pragma unroll
    for (int c = 0; c < 2; ++c)
#pragma unroll
      for (int qd = 0; qd < 4; ++qd) {
        const f4 v = {acc[a][c][4 * qd], acc[a][c][4 * qd + 1], acc[a][c][4 * qd + 2], acc[a][c][4 * qd + 3]};
        *(f4*)(red + (w * 64 + 32 * a + (l & 31)) * G2_RS + 32 * c + 8 * qd + 4 * (l >> 5)) = v;
      }
  __syncthreads();
#pragma unroll
  for (int it = 0; it < 2; ++it) {
    const int row = row0 + 32 * it;
    f4 s0 = {0.f, 0.f, 0.f, 0.f}, s1v = {0.f, 0.f, 0.f, 0.f};
#pragma unroll
    for (int ww = 0; ww < 4; ++ww) {
      const float* p = red + (ww * 64 + row) * G2_RS + 8 * oct;
      s0 += *(const f4*)p;
      s1v += *(const f4*)(p + 4);
    }
    u4 o;
#pragma unroll
    for (int e = 0; e < 4; ++e) {
      const f4 sv = (e < 2) ? s0 : s1v, gv = (e < 2) ? gb0 : gb1;
      const unsigned yb = pkbf(sv[(2 * e) & 3], sv[(2 * e + 1) & 3]);
      const unsigned gy = pkbf(bflo(yb) * gv[(2 * e) & 3], bfhi(yb) * gv[(2 * e + 1) & 3]);
      o[e] = pkbf(bflo(xr[it][e]) + bflo(gy), bfhi(xr[it][e]) + bfhi(gy));
    }
    const int rp = 64 * rt + row;
    const int s = 16 * (rp & 127) + (rp >> 7);
    *(u4*)(out + (size_t)s * 512 + dcol) = o;
  }
}

extern "C" void kernel_launch(void* const* d_in, const int* in_sizes, int n_in, void* d_out, int out_size,
                              void* d_ws, size_t ws_size, hipStream_t stream) {
  const u16* x = (const u16*)d_in[0];
  const u16* wr = (const u16*)d_in[1];
  const u16* wi = (const u16*)d_in[2];
  const u16* wg = (const u16*)d_in[4];
  const u16* wu = (const u16*)d_in[5];
  const u16* wd = (const u16*)d_in[6];
  const u16* cv = (const u16*)d_in[7];
  const u16* wm = (const u16*)d_in[8];
  char* ws = (char*)d_ws;
  constexpr int G2_LDS = 4 * 64 * G2_RS * 4;
  (void)hipFuncSetAttribute((const void*)kG1, hipFuncAttributeMaxDynamicSharedMemorySize, G1_LDS);
  (void)hipFuncSetAttribute((const void*)kG2, hipFuncAttributeMaxDynamicSharedMemorySize, G2_LDS);
  kA<<<KA_MAIN + KA_GEMV + KA_WPREP + KA_W2, 512, 0, stream>>>(x, wr, wi, wd, cv, wm, ws);
  kB<<<KB_MAIN + KB_W1, 512, 0, stream>>>(wg, wu, ws);
  kC<<<256, 512, 0, stream>>>(x, ws);
  kG1<<<240, 256, G1_LDS, stream>>>(ws);
  kG2<<<256, 256, G2_LDS, stream>>>(ws, (u16*)d_out);
}
```

```cpp
#define WARM2 0
#include <hip/hip_runtime.h>
#include <hip/hip_bf16.h>
#include <stdint.h>
#include <utility>

typedef unsigned short u16;
typedef _Float16 h2 __attribute__((ext_vector_type(2)));
typedef _Float16 h4 __attribute__((ext_vector_type(4)));
typedef _Float16 h8 __attribute__((ext_vector_type(8)));
typedef __bf16 b2 __attribute__((ext_vector_type(2)));
typedef __bf16 b8 __attribute__((ext_vector_type(8)));
typedef float f2 __attribute__((ext_vector_type(2)));
typedef float f4 __attribute__((ext_vector_type(4)));
typedef float f16v __attribute__((ext_vector_type(16)));
typedef unsigned u2 __attribute__((ext_vector_type(2)));
typedef unsigned u4 __attribute__((ext_vector_type(4)));
typedef unsigned u4a __attribute__((ext_vector_type(4), aligned(4)));
typedef short s4 __attribute__((ext_vector_type(4)));
#define DI __device__ __forceinline__
#define LDS3 __attribute__((address_space(3)))

constexpr int FH = 1376;
constexpr size_t OFF_MOD = 0;
constexpr size_t OFF_MMAX = 12288;
constexpr size_t OFF_BETA = 12544;
constexpr size_t OFF_YSC = 12800 + 256;
constexpr size_t OFF_MODT = 13312;
constexpr size_t OFF_Y = 20480;
constexpr size_t OFF_WP = OFF_Y + 2097152;
constexpr size_t OFF_V = OFF_WP + 65 * 32768;
constexpr size_t OFF_X1 = OFF_V + 2097152;
constexpr size_t OFF_ST = OFF_X1 + 2097152;
constexpr size_t OFF_W1 = OFF_ST + 262144;
constexpr size_t OFF_W2 = OFF_W1 + 15 * 33 * 6144;
constexpr size_t OFF_H = OFF_W2 + 16 * 86 * 1024;

#define STAMP_DECL
#define STAMP(idx)
#define STAMPC(idx)
#define STAMPK
#define STAMPEND(kid)
#define STAMP_ARG
#define STAMP_PASS

DI float bf2f(unsigned v) { return __builtin_bit_cast(float, v << 16); }
DI float bflo(unsigned v) { return __builtin_bit_cast(float, v << 16); }
DI float bfhi(unsigned v) { return __builtin_bit_cast(float, v & 0xffff0000u); }
DI unsigned pkbf(float a, float b) { f2 v = {a, b}; return __builtin_bit_cast(unsigned, __builtin_convertvector(v, b2)); }
DI unsigned pkh(float a, float b) { f2 v = {a, b}; return __builtin_bit_cast(unsigned, __builtin_convertvector(v, h2)); }
DI float rbf(float a) { return bflo(pkbf(a, a)); }
DI float cosr(float rev) { return __builtin_amdgcn_cosf(rev); }
DI float sinr(float rev) { return __builtin_amdgcn_sinf(rev); }
DI b2 asB2(unsigned v) { return __builtin_bit_cast(b2, v); }
DI h2 asH2(unsigned v) { return __builtin_bit_cast(h2, v); }
DI unsigned bfi(unsigned m, unsigned a, unsigned b) { return (m & a) | (~m & b); }
DI h8 asH(u4 v) { return __builtin_bit_cast(h8, v); }
DI b8 asB(u4 v) { return __builtin_bit_cast(b8, v); }
DI f4 mm16(h8 a, h8 b, f4 c) { return __builtin_amdgcn_mfma_f32_16x16x32_f16(a, b, c, 0, 0, 0); }
DI f16v mm32h(h8 a, h8 b, f16v c) { return __builtin_amdgcn_mfma_f32_32x32x16_f16(a, b, c, 0, 0, 0); }
DI f16v mm32b(b8 a, b8 b, f16v c) { return __builtin_amdgcn_mfma_f32_32x32x16_bf16(a, b, c, 0, 0, 0); }
DI h8 pack8(f4 r, f4 i) { u4 v = {pkh(r[0], r[1]), pkh(r[2], r[3]), pkh(i[0], i[1]), pkh(i[2], i[3])}; return asH(v); }
template <int CTRL> DI float dppadd(float v) { return v + __builtin_amdgcn_update_dpp(0.f, v, CTRL, 0xf, 0xf, false); }
DI float sum16(float v) { v = dppadd<0x128>(v); v = dppadd<0x124>(v); v = dppadd<0x122>(v); return dppadd<0x121>(v); }
DI float wavemax(float v) {
  for (int o = 32; o; o >>= 1) v = fmaxf(v, __shfl_xor(v, o));
  return v;
}
DI unsigned scale_exp(float m) { unsigned e = (__builtin_bit_cast(unsigned, m) >> 23) & 0xff; return min(max(e, 1u), 252u); }
DI float scale_up(unsigned e) { return __builtin_bit_cast(float, (e + 1) << 23); }
DI float scale_inv(unsigned e) { return __builtin_bit_cast(float, (253 - e) << 23); }
typedef __attribute__((address_space(4))) const float cf32;
DI cf32* cptr(const char* p) { return (cf32*)(uintptr_t)p; }
typedef __attribute__((address_space(4))) const unsigned cu32;
template <int N> DI unsigned smax_bits(const char* p) {
  cu32* q = (cu32*)(uintptr_t)p;
  unsigned m = 0;
#pragma unroll
  for (int i = 0; i < N; ++i) m = max(m, q[i]);
  return m;
}

DI f2 lo2(f4 v) { return __builtin_shufflevector(v, v, 0, 1); }
DI f2 hi2(f4 v) { return __builtin_shufflevector(v, v, 2, 3); }
DI h8 frag4(unsigned a, unsigned b, unsigned c, unsigned d) { u4 v = {a, b, c, d}; return asH(v); }
DI s4 trread(const char* p) { return __builtin_amdgcn_ds_read_tr16_b64_v4i16((LDS3 s4*)p); }
DI h8 trfrag(const char* p0, const char* p1) {
  const u2 lo = __builtin_bit_cast(u2, trread(p0)), hi = __builtin_bit_cast(u2, trread(p1));
  return frag4(lo[0], lo[1], hi[0], hi[1]);
}
constexpr unsigned NEG2 = 0x80008000u;
#define WT 16
DI __amdgpu_buffer_rsrc_t wsr(char* ws) { return __builtin_amdgcn_make_buffer_rsrc(ws, 0, 0x4000000, 0x00020000); }
template <int AUX> DI void st16(char* ws, unsigned off, u4 v) { __builtin_amdgcn_raw_buffer_store_b128(v, wsr(ws), off, 0, AUX); }
template <int AUX> DI void st8(char* ws, unsigned off, u2 v) { __builtin_amdgcn_raw_buffer_store_b64(v, wsr(ws), off, 0, AUX); }
DI void cs4(float r0, float step, float sc, unsigned& c01, unsigned& c23, unsigned& s01, unsigned& s23) {
  float c[4], s[4];
#pragma unroll
  for (int i = 0; i < 4; ++i) {
    const float r = __builtin_amdgcn_fractf(r0 + step * i);
    c[i] = sc * cosr(r);
    s[i] = sc * sinr(r);
  }
  c01 = pkh(c[0], c[1]); c23 = pkh(c[2], c[3]); s01 = pkh(s[0], s[1]); s23 = pkh(s[2], s[3]);
}

constexpr int KA_MAIN = 128, KA_GEMV = 32, KA_WPREP = 65;
constexpr int KA_RPL = 16 * 136;
constexpr int KA_RAW = 8 * KA_RPL + 4 * (KA_RPL + 32);
constexpr int KA_STAT = KA_RAW + 128 * 128, KA_LDS = KA_STAT + 1024;

DI void kA_dft(u4 (&bfr)[8], int m, int h) {
  const int slot = m >> 1, keff = (m == 1) ? 64 : slot;
  const float st = (float)keff * (1.f / 128.f), ph = ((m & 1) && slot) ? 0.25f : 0.f;
#pragma unroll
  for (int k0 = 0; k0 < 2; ++k0) {
    const float r0 = st * (16 * k0 + 8 * h) + ph;
    u4 cc, ss;
    unsigned a0, a1, a2, a3;
    cs4(r0, st, 0.08838834764831845f, a0, a1, a2, a3);
    cc[0] = a0; cc[1] = a1; ss[0] = a2; ss[1] = a3;
    cs4(r0 + 4.f * st, st, 0.08838834764831845f, a0, a1, a2, a3);
    cc[2] = a0; cc[3] = a1; ss[2] = a2; ss[3] = a3;
    bfr[k0] = cc;
#pragma unroll
    for (int kq = 1; kq < 4; ++kq) {
      const int q = (keff * kq) & 3;
      const unsigned sg = ((q + 1) & 2) ? NEG2 : 0u;
#pragma unroll
      for (int e = 0; e < 4; ++e) bfr[2 * kq + k0][e] = ((q & 1) ? ss[e] : cc[e]) ^ sg;
    }
  }
  asm volatile("" : "+v"(bfr[0]), "+v"(bfr[1]), "+v"(bfr[2]), "+v"(bfr[3]), "+v"(bfr[4]), "+v"(bfr[5]), "+v"(bfr[6]), "+v"(bfr[7]));
}

DI void kA_main(const u16* __restrict__ x, char* ws, char* smem, int b, int w, int l STAMP_ARG) {
  const int s2 = (b & 7) * 2 + ((b >> 3) & 1), c = b >> 4;
  const int i16 = l & 15, g = l >> 4, n32 = l & 31, h = l >> 5;
  const int rg = (w + c) & 7;
  const int r4 = (l >> 2) & 3, pc = l & 3;
  u4 bfr[8], xv[4][4];
  if (w >= 4) kA_dft(bfr, 32 * (w & 3) + n32, h);
  __builtin_amdgcn_sched_barrier(0);
  {
    const u16* xr = x + (size_t)(16 * (16 * rg + r4) + s2) * 512 + 32 * g + 8 * pc;
#pragma unroll
    for (int G = 0; G < 4; ++G)
#pragma unroll
      for (int q = 0; q < 4; ++q) xv[G][q] = *(const u4*)(xr + G * (64 * 512) + 128 * ((q + w) & 3));
  }
  __builtin_amdgcn_sched_barrier(0);
  STAMP(13);
  if (w < 4) kA_dft(bfr, 32 * (w & 3) + n32, h);
  unsigned mh = (c & 1) ? ~0u : 0u;
  asm volatile("" : "+s"(mh));
  __builtin_amdgcn_sched_barrier(0);
  STAMP(1);

  char* raw = smem + w * KA_RPL;
  char* rawp = ((pc == (c >> 1)) ? raw : smem + 8 * KA_RPL + pc * (KA_RPL + 32)) + r4 * 136 + g * 8;
  float* stat = (float*)(smem + KA_STAT) + w * 32;
  const u4 ones = {0x3f803f80u, 0x3f803f80u, 0x3f803f80u, 0x3f803f80u};
#pragma unroll
  for (int G = 0; G < 4; ++G) {
    f4 as = {0.f, 0.f, 0.f, 0.f}, aq = {0.f, 0.f, 0.f, 0.f};
#pragma unroll
    for (int q = 0; q < 4; ++q) {
      as = __builtin_amdgcn_mfma_f32_16x16x32_bf16(asB(xv[G][q]), asB(ones), as, 0, 0, 0);
      aq = __builtin_amdgcn_mfma_f32_16x16x32_bf16(asB(xv[G][q]), asB(xv[G][q]), aq, 0, 0, 0);
      *(u2*)(rawp + G * (4 * 136) + ((q + w) & 3) * 32) = u2{bfi(mh, xv[G][q][2], xv[G][q][0]), bfi(mh, xv[G][q][3], xv[G][q][1])};
    }
    float dq = (pc & 2) ? ((pc & 1) ? aq[3] : aq[2]) : ((pc & 1) ? aq[1] : aq[0]);
    dq = dppadd<0xB1>(dq);
    dq = dppadd<0x4E>(dq);
    if (i16 == 4 * g) *(f2*)(stat + (4 * G + g) * 2) = f2{(as[0] + as[1]) + (as[2] + as[3]), dq};
  }
  STAMP(2);
  const int row = l >> 2;
  u2 rv[4];
#pragma unroll
  for (int i = 0; i < 4; ++i) rv[i] = *(const u2*)(raw + row * 136 + pc * 32 + i * 8);
  const f2 sv = *(const f2*)(stat + row * 2);
  const float mean = sv[0] * (1.f / 512.f);
  const float var = fmaxf(sv[1] * (1.f / 512.f) - mean * mean, 0.f);
  const float sd = rbf(__builtin_amdgcn_sqrtf(rbf(rbf(var) + 1e-5f)));
  const float rs = __builtin_amdgcn_rcpf(sd);
  const float nb = -rbf(mean) * rs;
  char* nimg = smem + KA_RAW;
  {
    const int s1 = 16 * rg + row;
    char* nb_ = nimg + s1 * 128 + pc * 32;
    const int sw = s1 & 2;
#pragma unroll
    for (int tp = 0; tp < 2; ++tp) {
      u4 o;
#pragma unroll
      for (int t = 0; t < 2; ++t) {
        float v[4];
#pragma unroll
        for (int i = 0; i < 4; ++i) v[i] = fmaf(t ? bfhi(rv[i][tp]) : bflo(rv[i][tp]), rs, nb);
        o[2 * t] = pkh(v[0], v[1]);
        o[2 * t + 1] = pkh(v[2], v[3]);
      }
      *(u4*)(nb_ + (((2 * tp) ^ sw) * 8)) = o;
    }
  }
  STAMP(3);
  __syncthreads();
  STAMP(4);
  {
    const int q = i16 >> 2, p4 = i16 & 3, tp = w >> 2;
    const char* a = nimg + (8 * h + q) * 128 + p4 * 32 + (((2 * tp + (g & 1)) ^ (q & 2)) * 8);
    f16v acc;
#pragma unroll
    for (int e = 0; e < 16; ++e) acc[e] = 0.f;
#pragma unroll
    for (int ks = 0; ks < 8; ++ks) acc = mm32h(trfrag(a + ks * 2048, a + ks * 2048 + 512), asH(bfr[ks]), acc);
    char* yb = raw + (n32 >> 1) * 136 + (n32 & 1) * 32 + h * 8;
#pragma unroll
    for (int qd = 0; qd < 4; ++qd)
      *(u2*)(yb + (qd >> 1) * 64 + (qd & 1) * 16) = u2{pkh(acc[4 * qd], acc[4 * qd + 1]), pkh(acc[4 * qd + 2], acc[4 * qd + 3])};
    STAMP(5);
#pragma unroll
    for (int i = 0; i < 2; ++i) {
      const int sl = 8 * i + (l >> 3);
      const char* p = raw + sl * 136 + (l & 7) * 16;
      const u2 lo = *(const u2*)p, hi = *(const u2*)(p + 8);
      st16<WT>(ws, OFF_Y + (16 * (w & 3) + sl) * 32768 + s2 * 2048 + c * 256 + tp * 128 + (l & 7) * 16, u4{lo[0], lo[1], hi[0], hi[1]});
    }
  }
}

DI void mod_gemv(const u16* __restrict__ cvec, const u16* __restrict__ wmod, char* ws, char* smem, int bb, int tid) {
  const int oct = tid & 7, rg = tid >> 3, l = tid & 63, w = tid >> 6;
  u4 wv[8];
  u16 cv[8];
#pragma unroll
  for (int i = 0; i < 8; ++i) {
    cv[i] = cvec[rg + 64 * i];
    wv[i] = *(const u4*)(wmod + (size_t)(rg + 64 * i) * 3072 + 64 * bb + 8 * oct);
  }
  float acc[8];
#pragma unroll
  for (int e = 0; e < 8; ++e) acc[e] = 0.f;
#pragma unroll
  for (int i = 0; i < 8; ++i) {
    const float cf = bf2f(cv[i]);
    const float sl = rbf(cf / (1.f + __expf(-cf)));
#pragma unroll
    for (int e = 0; e < 4; ++e) {
      acc[2 * e] = fmaf(sl, bflo(wv[i][e]), acc[2 * e]);
      acc[2 * e + 1] = fmaf(sl, bfhi(wv[i][e]), acc[2 * e + 1]);
    }
  }
  float* red = (float*)smem;
  *(f4*)(red + rg * 68 + 8 * oct) = f4{acc[0], acc[1], acc[2], acc[3]};
  *(f4*)(red + rg * 68 + 8 * oct + 4) = f4{acc[4], acc[5], acc[6], acc[7]};
  __syncthreads();
  float v = 0.f;
#pragma unroll
  for (int i = 0; i < 8; ++i) v += red[(8 * w + i) * 68 + l];
  float* red2 = red + 64 * 68;
  red2[w * 64 + l] = v;
  __syncthreads();
  if (tid < 64) {
    v = 0.f;
#pragma unroll
    for (int i = 0; i < 8; ++i) v += red2[i * 64 + tid];
    v = rbf(v);
    const int sec = bb >> 3;
    if (sec == 1 || sec == 4) v = rbf(1.f + v);
    ((float*)(ws + OFF_MOD))[64 * bb + tid] = v;
    if (sec < 2) {
      const int d = 64 * (bb & 7) + tid;
      ((float*)(ws + OFF_MODT))[sec * 512 + (d & 31) * 16 + (d >> 5)] = v;
      const float mx = wavemax(fabsf(v));
      if (tid == 0) ((float*)(ws + OFF_MMAX))[bb] = mx;
    }
  }
}

DI void kA_wprep(const u16* __restrict__ wr, const u16* __restrict__ wi, char* ws, char* smem, int k1, int tid) {
  u4 tv[4];
#pragma unroll
  for (int it = 0; it < 4; ++it) {
    const int i = tid + 512 * it, row = i >> 5, pc = i & 31;
    const int ksA = k1 + 128 * (row & 15);
    const int ks = (row >> 5) ? ((2048 - ksA) & 2047) : ksA;
    tv[it] = *(const u4a*)((const char*)(((row >> 4) & 1) ? wi : wr) + ((ks * 514) & ~3) + pc * 16);
  }
  if (tid < 64) {
    const int row = tid;
    const int ksA = k1 + 128 * (row & 15);
    const int ks = (row >> 5) ? ((2048 - ksA) & 2047) : ksA;
    const char* src = (const char*)(((row >> 4) & 1) ? wi : wr) + ((ks * 514) & ~3) + 512;
    *(unsigned*)(smem + row * 528 + 512) = *(const unsigned*)src;
  }
#pragma unroll
  for (int it = 0; it < 4; ++it) {
    const int i = tid + 512 * it;
    *(u4*)(smem + (i >> 5) * 528 + (i & 31) * 16) = tv[it];
  }
  __syncthreads();
  const int mis = (k1 & 1) * 2;
  const int l = tid & 63, k2 = l & 15, q = l >> 4, hh = (tid >> 6) & 1;
  const char* base = smem + (hh * 32 + k2) * 528 + mis;
  const float sgn = hh ? -1.f : 1.f;
  float re[4][4], im[4][4], mx = 0.f;
#pragma unroll
  for (int it = 0; it < 4; ++it) {
    const int j1 = (tid >> 7) + 4 * it;
#pragma unroll
    for (int r = 0; r < 4; ++r) {
      const int kd = j1 + 16 * (16 * hh + 4 * q + r);
      const int idx = hh ? 512 - kd : kd;
      re[it][r] = bf2f(*(const u16*)(base + idx * 2));
      im[it][r] = sgn * bf2f(*(const u16*)(base + idx * 2 + 16 * 528));
    }
  }
  if (tid < 128 && q == 0) {
    const char* ob = smem + ((hh ^ 1) * 32 + k2) * 528 + mis + hh * 512;
    re[0][0] = 0.5f * (re[0][0] + bf2f(*(const u16*)ob));
    im[0][0] = 0.5f * (im[0][0] - sgn * bf2f(*(const u16*)(ob + 16 * 528)));
  }
#pragma unroll
  for (int it = 0; it < 4; ++it)
#pragma unroll
    for (int r = 0; r < 4; ++r) mx = fmaxf(mx, fmaxf(fabsf(re[it][r]), fabsf(im[it][r])));
  mx = wavemax(mx);
  float* red = (float*)(smem + 64 * 528);
  if (l == 0) red[tid >> 6] = mx;
  __syncthreads();
#pragma unroll
  for (int i = 0; i < 8; ++i) mx = fmaxf(mx, red[i]);
  const unsigned e = scale_exp(mx);
  const float inv = scale_inv(e);
  if (tid == 0) ((float*)(ws + OFF_BETA))[k1] = scale_up(e);
#pragma unroll
  for (int it = 0; it < 4; ++it) {
    const u4 o = {pkh(re[it][0] * inv, re[it][1] * inv), pkh(re[it][2] * inv, re[it][3] * inv),
                  pkh(im[it][0] * inv, im[it][1] * inv), pkh(im[it][2] * inv, im[it][3] * inv)};
    st16<WT>(ws, OFF_WP + k1 * 32768 + (tid + 512 * it) * 16, o);
  }
}

DI void pack_w2(const u16* __restrict__ wd, char* ws, char* smem, int kb, int tid) {
  u4 tv[4];
#pragma unroll
  for (int it = 0; it < 4; ++it) {
    const int i = tid + 512 * it;
    tv[it] = *(const u4*)(wd + (size_t)(32 * kb + (i >> 6)) * 512 + (i & 63) * 8);
  }
#pragma unroll
  for (int it = 0; it < 4; ++it) {
    const int i = tid + 512 * it;
    *(u4*)(smem + (i >> 6) * 1088 + (i & 63) * 16) = tv[it];
  }
  __syncthreads();
  const int l = tid & 63, w = tid >> 6, h = l >> 5, gi = (l >> 4) & 1, q = (l & 15) >> 2, p = l & 3;
#pragma unroll
  for (int it = 0; it < 4; ++it) {
    const int nt = w + 8 * (it & 1), kk = it >> 1;
    const char* a = smem + (16 * kk + 8 * h + q) * 1088 + (32 * nt + 16 * gi + 4 * p) * 2;
    st16<WT>(ws, OFF_W2 + (nt * 86 + 2 * kb + kk) * 1024 + l * 16, __builtin_bit_cast(u4, trfrag(a, a + 4 * 1088)));
  }
}

__global__ __launch_bounds__(512) void kA(const u16* __restrict__ x, const u16* __restrict__ wr,
                                          const u16* __restrict__ wi, const u16* __restrict__ cvec,
                                          const u16* __restrict__ wmod, char* ws) {
  __shared__ __attribute__((aligned(16))) char smem[KA_LDS];
  const int b = blockIdx.x, tid = threadIdx.x;
  STAMP_DECL;
  STAMP(0);
  STAMPK;
  if (b < KA_MAIN) kA_main(x, ws, smem, b, __builtin_amdgcn_readfirstlane(tid >> 6), tid & 63 STAMP_PASS);
  else if (b < KA_MAIN + KA_GEMV) mod_gemv(cvec, wmod, ws, smem, (b - KA_MAIN) + ((b - KA_MAIN) >= 16 ? 8 : 0), tid);
  else kA_wprep(wr, wi, ws, smem, b - KA_MAIN - KA_GEMV, tid);
  STAMPEND(0);
}

constexpr int KB_MAIN = 65, KB_W1 = 88, KB_W2 = 43, KB_GEMV = 16;
constexpr int T1_BYTES = 16 * 2064, T2_BYTES = 32768;
constexpr int KB_TAB = T1_BYTES + T2_BYTES, KB_MOD = KB_TAB + 24 * 1024, KB_LDS = KB_MOD + 2048;

DI void cmulc(f2& r, f2& i, float tc, float ts) {
  const f2 a = r * tc + i * ts;
  i = i * tc - r * ts;
  r = a;
}

DI void kB_main(char* ws, char* smem, int k1, int w, int l STAMP_ARG) {
  const int i16 = l & 15, g = l >> 4;
  const int slot = k1 & 63;
  const bool realonly = (slot == 0);
  u4 yv[4], wv[2][2];
  {
    const char* yb = ws + OFF_Y + (size_t)slot * 32768 + g * 2048 + w * 256 + i16 * 16;
#pragma unroll
    for (int i = 0; i < 4; ++i) yv[i] = *(const u4*)(yb + i * 8192);
  }
  const f2 mraw = *(const f2*)(ws + OFF_MODT + (w * 64 + l) * 8);
  const unsigned ae = scale_exp(__builtin_bit_cast(float, smax_bits<16>(ws + OFF_MMAX)));
  const unsigned bmax = smax_bits<65>(ws + OFF_BETA), bk = ((cu32*)(uintptr_t)(ws + OFF_BETA))[k1];
  __builtin_amdgcn_sched_barrier(0);
  STAMP(13);

  char* T1 = smem;
  char* T2 = smem + T1_BYTES;
  char* tab = smem + KB_TAB + l * 16;
  const int n32 = l & 31, h = l >> 5, ro = n32 >> 4, d1 = n32 & 15;
  {
    unsigned c01, c23, s01, s23;
    if (w < 4) {
      const float st = (float)(16 * (w >> 1) + i16) * (1.f / 32.f);
      cs4(st * (16 * (w & 1) + 4 * g), st, 0.17677669529663687f, c01, c23, s01, s23);
      char* t = tab + w * 4096;
      *(u4*)t = u4{c01, c23, s01, s23};
      *(u4*)(t + 1024) = u4{s01 ^ NEG2, s23 ^ NEG2, c01, c23};
      *(u4*)(t + 2048) = u4{c01, c23, s01 ^ NEG2, s23 ^ NEG2};
      *(u4*)(t + 3072) = u4{s01, s23, c01, c23};
    } else if (w < 6) {
      const float st = (float)i16 * (1.f / 16.f);
      cs4(st * (8 * (g & 1) + 4 * (w & 1)), st, 0.25f, c01, c23, s01, s23);
      char* t = tab + 16 * 1024 + (w & 1) * 8;
      *(u2*)t = (g < 2) ? u2{c01, c23} : u2{s01, s23};
      *(u2*)(t + 1024) = (g < 2) ? u2{s01 ^ NEG2, s23 ^ NEG2} : u2{c01, c23};
      const float s3 = (float)d1 * (1.f / 16.f);
      cs4(s3 * (4 * (2 * (w & 1) + h)), s3, 0.25f, c01, c23, s01, s23);
      *(u4*)(tab + (22 + (w & 1)) * 1024) = ro ? u4{s01, c01, s23, c23} : u4{c01, s01 ^ NEG2, c23, s23 ^ NEG2};
    } else if (w == 6) {
      const float st = (float)(k1 + 128 * i16) * (1.f / 2048.f);
      cs4(st * (4 * g), st, 0.25f, c01, c23, s01, s23);
      *(u4*)(tab + 18 * 1024) = u4{c01, c23, s01, s23};
      *(u4*)(tab + 19 * 1024) = u4{s01 ^ NEG2, s23 ^ NEG2, c01, c23};
    } else {
      cs4((float)(i16 * (k1 + 512 * g)) * (1.f / 2048.f), (float)i16 * (1.f / 16.f), 0.25f, c01, c23, s01, s23);
      *(u4*)(tab + 20 * 1024) = u4{c01, c23, s01 ^ NEG2, s23 ^ NEG2};
      *(u4*)(tab + 21 * 1024) = u4{s01, s23, c01, c23};
    }
  }
#pragma unroll
  for (int t = 0; t < 2; ++t)
#pragma unroll
    for (int hh = 0; hh < 2; ++hh)
      wv[t][hh] = *(const u4*)(ws + OFF_WP + (size_t)k1 * 32768 + (((2 * w + t) * 2 + hh) * 64 + l) * 16);
  float tc1[4], ts1[4], tc2[2][2], ts2[2][2];
#pragma unroll
  for (int t = 0; t < 4; ++t) {
    const float r = (float)((4 * w + t) * i16) * (1.f / 512.f);
    tc1[t] = cosr(r);
    ts1[t] = sinr(r);
    const float r2 = (float)((16 * (t & 1) + i16) * (2 * w + (t >> 1))) * (1.f / 512.f);
    tc2[t >> 1][t & 1] = cosr(r2);
    ts2[t >> 1][t & 1] = -sinr(r2);
  }
  asm volatile("" : "+v"(tc1[0]), "+v"(tc1[1]), "+v"(tc1[2]), "+v"(tc1[3]), "+v"(ts1[0]), "+v"(ts1[1]), "+v"(ts1[2]), "+v"(ts1[3]));
  asm volatile("" : "+v"(tc2[0][0]), "+v"(tc2[0][1]), "+v"(tc2[1][0]), "+v"(tc2[1][1]), "+v"(ts2[0][0]), "+v"(ts2[0][1]), "+v"(ts2[1][0]), "+v"(ts2[1][1]));
  __builtin_amdgcn_sched_barrier(0);
  STAMP(1);
  char* yb = T2 + w * 4096;
#pragma unroll
  for (int i = 0; i < 4; ++i) *(u4*)(yb + (4 * i + g) * 256 + ((i16 ^ (4 * i + g)) * 16)) = yv[i];
  {
    const float sc = scale_inv(ae) * (w < 4 ? 11.313708498984761f : 1.f);
    *(unsigned*)(smem + KB_MOD + (w * 64 + l) * 4) = pkh(mraw[0] * sc, mraw[1] * sc);
  }
  __syncthreads();
  STAMP(2);

  const f4 z = {0.f, 0.f, 0.f, 0.f};
  h8 f32t[16], ghre, ghim, if16[2];
  {
    const int go = realonly ? ((g & 1) + (k1 >> 6) * 2) : g;
    h8 a[4], mv[4];
    const char* mp = smem + KB_MOD + w * 128 + (g & 1) * 16;
#pragma unroll
    for (int t = 0; t < 4; ++t) {
      a[t] = *(const h8*)(yb + i16 * 256 + (((4 * t + go) ^ i16) * 16));
      mv[t] = *(const h8*)(mp + 1024 + t * 32);
    }
    const h8 f16re = *(const h8*)(tab + 16 * 1024), f16im = *(const h8*)(tab + 17 * 1024);
    const h8 gre = *(const h8*)(tab + 18 * 1024), gim = *(const h8*)(tab + 19 * 1024);
#pragma unroll
    for (int t = 0; t < 4; ++t) a[t] *= mv[t];
    if (realonly) {
#pragma unroll
      for (int t = 0; t < 4; ++t) {
        if (k1 == 0) a[t] += *(const h8*)(mp + t * 32);
        if (g >= 2) a[t] = asH(u4{0u, 0u, 0u, 0u});
      }
    }
    __builtin_amdgcn_sched_barrier(0);
    f4 dr[4], di[4];
#pragma unroll
    for (int t = 0; t < 4; ++t) { dr[t] = mm16(a[t], f16re, z); di[t] = mm16(a[t], f16im, z); }
#pragma unroll
    for (int i = 0; i < 16; ++i) f32t[i] = *(const h8*)(tab + i * 1024);
    ghre = *(const h8*)(tab + 20 * 1024);
    ghim = *(const h8*)(tab + 21 * 1024);
    if16[0] = *(const h8*)(tab + 22 * 1024);
    if16[1] = *(const h8*)(tab + 23 * 1024);
    __builtin_amdgcn_sched_barrier(0);
#pragma unroll
    for (int t = 0; t < 4; ++t) {
      const h8 xb = pack8(dr[t], di[t]);
      dr[t] = mm16(gre, xb, z);
      di[t] = mm16(gim, xb, z);
    }
    __builtin_amdgcn_sched_barrier(0);
    f2 pr[4][2], pi[4][2];
#pragma unroll
    for (int t = 0; t < 4; ++t) {
      pr[t][0] = lo2(dr[t]); pr[t][1] = hi2(dr[t]); pi[t][0] = lo2(di[t]); pi[t][1] = hi2(di[t]);
      cmulc(pr[t][0], pi[t][0], tc1[t], ts1[t]);
      cmulc(pr[t][1], pi[t][1], tc1[t], ts1[t]);
    }
#pragma unroll
    for (int e = 0; e < 4; ++e) {
      const int k2 = 4 * g + e;
      const u4 o = {pkh(pr[0][e >> 1][e & 1], pr[1][e >> 1][e & 1]), pkh(pr[2][e >> 1][e & 1], pr[3][e >> 1][e & 1]),
                    pkh(pi[0][e >> 1][e & 1], pi[1][e >> 1][e & 1]), pkh(pi[2][e >> 1][e & 1], pi[3][e >> 1][e & 1])};
      *(u4*)(T1 + i16 * 2064 + k2 * 128 + ((w ^ ((k2 >> 1) & 7)) * 16)) = o;
    }
  }
  STAMP(3);
  __syncthreads();
  STAMP(4);

  {
    h8 b0[2], b1[2], za[2][2];
#pragma unroll
    for (int t = 0; t < 2; ++t) {
      const char* tb = T1 + (2 * w + t) * 2064 + i16 * 128;
      b0[t] = *(const h8*)(tb + ((g ^ ((i16 >> 1) & 7)) * 16));
      b1[t] = *(const h8*)(tb + (((4 + g) ^ ((i16 >> 1) & 7)) * 16));
    }
    f4 xr[2][2], xi[2][2];
#pragma unroll
    for (int t = 0; t < 2; ++t)
#pragma unroll
      for (int hh = 0; hh < 2; ++hh) {
        xr[t][hh] = mm16(f32t[8 * hh], b0[t], z);
        xi[t][hh] = mm16(f32t[8 * hh + 1], b0[t], z);
      }
#pragma unroll
    for (int t = 0; t < 2; ++t)
#pragma unroll
      for (int hh = 0; hh < 2; ++hh) {
        xr[t][hh] = mm16(f32t[8 * hh + 4], b1[t], xr[t][hh]);
        xi[t][hh] = mm16(f32t[8 * hh + 5], b1[t], xi[t][hh]);
      }
    __builtin_amdgcn_sched_barrier(0);
#pragma unroll
    for (int t = 0; t < 2; ++t) {
#pragma unroll
      for (int hh = 0; hh < 2; ++hh) {
        const h8 xp = pack8(xr[t][hh], xi[t][hh]), wq = asH(wv[t][hh]);
        const h4 r4 = __builtin_shufflevector(xp, xp, 0, 1, 2, 3), i4 = __builtin_shufflevector(xp, xp, 4, 5, 6, 7);
        const h4 wr4 = __builtin_shufflevector(wq, wq, 0, 1, 2, 3), wi4 = __builtin_shufflevector(wq, wq, 4, 5, 6, 7);
        const h4 zr = __builtin_elementwise_fma(r4, wr4, -(i4 * wi4)), zi = __builtin_elementwise_fma(r4, wi4, i4 * wr4);
        za[t][hh] = __builtin_shufflevector(zr, zi, 0, 1, 2, 3, 4, 5, 6, 7);
      }
#pragma unroll
      for (int dh = 0; dh < 2; ++dh) {
        xr[t][dh] = mm16(za[t][1], f32t[8 * dh + 6], mm16(za[t][0], f32t[8 * dh + 2], z));
        xi[t][dh] = mm16(za[t][1], f32t[8 * dh + 7], mm16(za[t][0], f32t[8 * dh + 3], z));
      }
      __builtin_amdgcn_sched_barrier(0);
    }
#pragma unroll
    for (int dh = 0; dh < 2; ++dh)
#pragma unroll
      for (int t = 0; t < 2; ++t) {
        const h8 ub = pack8(xr[t][dh], xi[t][dh]);
        xr[t][dh] = mm16(ghre, ub, z);
        xi[t][dh] = mm16(ghim, ub, z);
      }
    __builtin_amdgcn_sched_barrier(0);
#pragma unroll
    for (int dh = 0; dh < 2; ++dh) {
      f2 qr[2][2], qi[2][2];
#pragma unroll
      for (int t = 0; t < 2; ++t) {
        qr[t][0] = lo2(xr[t][dh]); qr[t][1] = hi2(xr[t][dh]); qi[t][0] = lo2(xi[t][dh]); qi[t][1] = hi2(xi[t][dh]);
        cmulc(qr[t][0], qi[t][0], tc2[t][dh], ts2[t][dh]);
        cmulc(qr[t][1], qi[t][1], tc2[t][dh], ts2[t][dh]);
      }
      const int d2 = 16 * dh + i16;
#pragma unroll
      for (int e = 0; e < 4; ++e) {
        const u2 o = {pkh(qr[0][e >> 1][e & 1], qr[1][e >> 1][e & 1]), pkh(qi[0][e >> 1][e & 1], qi[1][e >> 1][e & 1])};
        *(u2*)(T2 + ((4 * g + e) * 32 + d2) * 64 + (((w >> 1) ^ ((d2 >> 2) & 3)) * 16) + (w & 1) * 8) = o;
      }
    }
  }
  {
    const unsigned de = bmax - bk;
    const _Float16 ratio = (_Float16)__builtin_bit_cast(float, de < 0x3f800000u ? 0x3f800000u - de : 0u);
    if16[0] *= ratio;
    if16[1] *= ratio;
  }
  if (k1 == 0 && w == 0 && l == 0) *(float*)(ws + OFF_YSC) = __builtin_bit_cast(float, bmax) * scale_up(ae);
  STAMP(5);
  __syncthreads();
  STAMP(6);

  const int krow = realonly ? (k1 >> 6) : (2 * slot + ro);
  const bool hswap = (slot >> 2) & 1;
  f16v acc[2];
#pragma unroll
  for (int t = 0; t < 2; ++t) {
    const char* tb = T2 + ((2 * w + t) * 32 + n32) * 64;
#pragma unroll
    for (int e = 0; e < 16; ++e) acc[t][e] = 0.f;
#pragma unroll
    for (int st = 0; st < 2; ++st) acc[t] = mm32h(*(const h8*)(tb + (((2 * st + h) ^ ((n32 >> 2) & 3)) * 16)), if16[st], acc[t]);
  }
#pragma unroll
  for (int t = 0; t < 2; ++t)
#pragma unroll
    for (int a = 0; a < 2; ++a) {
      const auto r0 = __builtin_amdgcn_permlane32_swap(pkh(acc[t][8 * a], acc[t][8 * a + 1]), pkh(acc[t][8 * a + 4], acc[t][8 * a + 5]), false, false);
      const auto r1 = __builtin_amdgcn_permlane32_swap(pkh(acc[t][8 * a + 2], acc[t][8 * a + 3]), pkh(acc[t][8 * a + 6], acc[t][8 * a + 7]), false, false);
      const u4 o = hswap ? u4{r0[1], r1[1], r0[0], r1[0]} : u4{r0[0], r1[0], r0[1], r1[1]};
      const unsigned off = OFF_V + ((2 * w + t) * 128 + krow) * 1024 + (2 * a + h) * 256 + d1 * 16;
      st16<WT>(ws, (realonly && ro) ? 0x7fffff00u : off, o);
    }
}

DI void pack_w1(const u16* __restrict__ wg, const u16* __restrict__ wu, char* ws, char* smem, int bb, int tid) {
  const int cg = bb >> 2, db = bb & 3;
  constexpr int RS = 192, MO = 128 * RS + 32, CO = 2 * MO + 2048;
  const int l = tid & 63, w = tid >> 6, r = l & 31, h = l >> 5;
  const int ft = w >> 1, kp = w & 1;
  {
    const int row = tid >> 3, oct = tid & 7;
    const int col = min(64 * cg + 8 * oct, FH - 8);
    u4 a[2], bq[2];
#pragma unroll
    for (int i = 0; i < 2; ++i) {
      const size_t o = (size_t)(128 * db + 64 * i + row) * FH + col;
      a[i] = *(const u4*)(wg + o);
      bq[i] = *(const u4*)(wu + o);
    }
    if (tid < 128) {
      const float* mod = (const float*)(ws + OFF_MOD) + 128 * db;
      *(float*)(smem + CO + tid * 4) = mod[2048 + tid];
      *(u16*)(smem + CO + 512 + tid * 2) = (u16)(__builtin_bit_cast(unsigned, mod[1536 + tid]) >> 16);
    }
#pragma unroll
    for (int i = 0; i < 2; ++i) {
      *(u4*)(smem + (64 * i + row) * RS + oct * 16) = a[i];
      *(u4*)(smem + MO + (64 * i + row) * RS + oct * 16) = bq[i];
    }
  }
  __syncthreads();
  const int T = 4 * cg + ft;
  const int chunk = T / 6, nt = T - 6 * chunk;
  const unsigned wb = OFF_W1 + chunk * 33 * 6144 + nt * 1024;
  const int q = (l & 15) >> 2, p = l & 3;
  const char* ta = smem + (p >> 1) * MO + (64 * kp + 8 * h + q) * RS + (16 * ft + 8 * (p & 1) + 4 * ((l >> 4) & 1)) * 2;
  const b2 ones = {(__bf16)1.0f, (__bf16)1.0f};
  float P = 0.f, Q = 0.f;
#pragma unroll
  for (int it = 0; it < 4; ++it) {
    const u4 v = __builtin_bit_cast(u4, trfrag(ta + 16 * it * RS, ta + (16 * it + 4) * RS));
    const int dl = 64 * kp + 16 * it + 8 * h;
    const f4 c0 = *(const f4*)(smem + CO + dl * 4), c1 = *(const f4*)(smem + CO + dl * 4 + 16);
    const u4 sb = *(const u4*)(smem + CO + 512 + dl * 2);
    const u4 o = {pkbf(bflo(v[0]) * c0[0], bfhi(v[0]) * c0[1]), pkbf(bflo(v[1]) * c0[2], bfhi(v[1]) * c0[3]),
                  pkbf(bflo(v[2]) * c1[0], bfhi(v[2]) * c1[1]), pkbf(bflo(v[3]) * c1[2], bfhi(v[3]) * c1[3])};
#pragma unroll
    for (int e = 0; e < 4; ++e) {
      P = __builtin_amdgcn_fdot2_f32_bf16(asB2(o[e]), ones, P, false);
      Q = __builtin_amdgcn_fdot2_f32_bf16(asB2(v[e]), asB2(sb[e]), Q, false);
    }
    if (T < 86) st16<WT>(ws, wb + (8 * db + 4 * kp + it) * 6144 + l * 16, o);
  }
  P += __shfl_xor(P, 32);
  Q += __shfl_xor(Q, 32);
  float* red = (float*)(smem + 2 * MO);
  if (l < 32) *(f2*)(red + (w * 32 + r) * 2) = f2{P, Q};
  __syncthreads();
  if (tid < 256) {
    const int pq = tid >> 7, ft2 = (tid >> 5) & 3, r2 = tid & 31;
    const float v = red[((2 * ft2) * 32 + r2) * 2 + pq] + red[((2 * ft2 + 1) * 32 + r2) * 2 + pq];
    const float hi = rbf(v);
    const int T2i = 4 * cg + ft2, ch2 = T2i / 6, nt2 = T2i - 6 * ch2;
    if (T2i < 86)
      *(unsigned*)(ws + OFF_W1 + (size_t)ch2 * 33 * 6144 + 32 * 6144 + nt2 * 1024 + (r2 + 32 * pq) * 16 + db * 4) = pkbf(hi, v - hi);
  }
}

__global__ __launch_bounds__(512) void kB(const u16* __restrict__ wg, const u16* __restrict__ wu, const u16* __restrict__ wd,
                                          const u16* __restrict__ cvec, const u16* __restrict__ wmod, char* ws) {
  extern __shared__ __attribute__((aligned(16))) char smem[];
  const int b = blockIdx.x, tid = threadIdx.x;
  STAMP_DECL;
  STAMP(0);
  STAMPK;
  if (b < KB_MAIN) kB_main(ws, smem, b, __builtin_amdgcn_readfirstlane(tid >> 6), tid & 63 STAMP_PASS);
  else if (b < KB_MAIN + KB_W1) pack_w1(wg, wu, ws, smem, b - KB_MAIN, tid);
  else if (b < KB_MAIN + KB_W1 + KB_GEMV) mod_gemv(cvec, wmod, ws, smem, (b - KB_MAIN - KB_W1) + ((b - KB_MAIN - KB_W1) >= 8 ? 32 : 16), tid);
  else pack_w2(wd, ws, smem, b - KB_MAIN - KB_W1 - KB_GEMV, tid);
  STAMPEND(1);
}

#define WT_X1 WT
__global__ __launch_bounds__(512) void kC(const u16* __restrict__ x, char* ws) {
  __shared__ __attribute__((aligned(16))) char smem[16384];
  const int b = blockIdx.x, tid = threadIdx.x;
  const int w = __builtin_amdgcn_readfirstlane(tid >> 6), l = tid & 63;
  STAMP_DECL;
  STAMP(0);
  STAMPK;
  const int s2 = (b & 7) * 2 + ((b >> 3) & 1), hs = (b >> 4) & 1, c8 = b >> 5;
  const int i16 = l & 15, g = l >> 4, st = w & 3, dh = w >> 2;
  const char* vsrc = ws + OFF_V + (size_t)(s2 * 128 + (tid >> 3)) * 1024 + c8 * 128 + (tid & 7) * 16;
  const u4 v0 = *(const u4*)vsrc, v1 = *(const u4*)(vsrc + 65536);
  const int s1 = 64 * hs + 16 * st + i16;
  const int d0 = 32 * (8 * (c8 & 1) + 4 * dh + g) + 8 * (c8 >> 1);
  const u4 xv = *(const u4*)(x + (size_t)(16 * s1 + s2) * 512 + d0);
  const float* mod = (const float*)(ws + OFF_MOD);
  const f4 ga0 = *(const f4*)(mod + 1024 + d0), ga1 = *(const f4*)(mod + 1024 + d0 + 4);
  const float ysc = cptr(ws + OFF_YSC)[0];
  __builtin_amdgcn_sched_barrier(0);

  u4 bfr[4];
  {
    float cv[4], sv[4];
#pragma unroll
    for (int e = 0; e < 4; ++e) {
      const float r = (float)((s1 * (4 * g + e)) & 127) * (1.f / 128.f);
      cv[e] = 0.1767766952966369f * cosr(r);
      sv[e] = -0.1767766952966369f * sinr(r);
    }
    const float rr = (float)(s1 & 7) * 0.125f, rc = cosr(rr), rsn = sinr(rr);
#pragma unroll
    for (int ks = 0; ks < 4; ++ks) {
#pragma unroll
      for (int e = 0; e < 4; ++e) {
        bfr[ks][e] = pkh(cv[e], sv[e]);
        if (ks < 3) {
          const float cn = cv[e] * rc + sv[e] * rsn;
          sv[e] = sv[e] * rc - cv[e] * rsn;
          cv[e] = cn;
        }
      }
    }
    if (g == 0) bfr[0][0] = pkh(0.08838834764831845f, (s1 & 1) ? -0.08838834764831845f : 0.08838834764831845f);
  }
  asm volatile("" : "+v"(bfr[0]), "+v"(bfr[1]), "+v"(bfr[2]), "+v"(bfr[3]));
  __builtin_amdgcn_sched_barrier(0);
  STAMP(1);
  {
    const int o = (tid * 16) ^ (((tid >> 4) & 1) * 64);
    *(u4*)(smem + o) = v0;
    *(u4*)(smem + 8192 + o) = v1;
  }
  __syncthreads();
  STAMP(2);

  const int q = i16 >> 2, p = i16 & 3;
  f4 acc[2];
#pragma unroll
  for (int t = 0; t < 2; ++t) {
    acc[t] = f4{0.f, 0.f, 0.f, 0.f};
#pragma unroll
    for (int ks = 0; ks < 4; ++ks) {
      const char* a = smem + (32 * ks + 8 * g + q) * 128 + (((32 * dh + 8 * p + 4 * t) * 2) ^ ((g & 1) * 8) ^ (((q >> 1) & 1) * 64));
      acc[t] = mm16(trfrag(a, a + 512), asH(bfr[ks]), acc[t]);
    }
  }
  u4 o;
  float sm = 0.f, sq = 0.f;
#pragma unroll
  for (int t = 0; t < 2; ++t)
#pragma unroll
    for (int e = 0; e < 2; ++e) {
      const f4 gv = (t ? ga1 : ga0) * ysc;
      const unsigned xx = xv[2 * t + e];
      const unsigned r = pkbf(fmaf(acc[t][2 * e], gv[2 * e], bflo(xx)), fmaf(acc[t][2 * e + 1], gv[2 * e + 1], bfhi(xx)));
      o[2 * t + e] = r;
      const float a = bflo(r), bq = bfhi(r);
      sm += a + bq;
      sq = fmaf(a, a, fmaf(bq, bq, sq));
    }
  st16<WT_X1>(ws, OFF_X1 + ((s2 * 4 + (s1 >> 5)) * 32 + (d0 >> 4)) * 1024 + ((s1 & 31) + 32 * ((d0 >> 3) & 1)) * 16, o);
  sm += __shfl_xor(sm, 16); sq += __shfl_xor(sq, 16);
  sm += __shfl_xor(sm, 32); sq += __shfl_xor(sq, 32);
  if (g == 0) st8<WT_X1>(ws, OFF_ST + ((s2 * 16 + 2 * c8 + dh) * 128 + s1) * 8, __builtin_bit_cast(u2, f2{sm, sq}));
  STAMPEND(2);
}

constexpr int G1_SLOT = 12288, G1_NSLOT = 6, G1_AHEAD = 4, G1_WARM = G1_SLOT * G1_NSLOT, G1_LDS = G1_WARM + G1_SLOT;

DI void ld1(u4& a, unsigned voff, const char* base) {
  asm volatile("global_load_dwordx4 %0, %1, %2" : "=v"(a) : "v"(voff), "s"(base) : "memory");
}
template <int OFF> DI void glds1(unsigned voff, const char* base, unsigned lds) {
  if constexpr (OFF == 0)
    asm volatile("s_mov_b32 m0, %2\n\ts_nop 0\n\tglobal_load_lds_dwordx4 %0, %1" :: "v"(voff), "s"(base), "s"(lds) : "memory");
  else
    asm volatile("global_load_lds_dwordx4 %0, %1 offset:%2" :: "v"(voff), "s"(base), "i"(OFF) : "memory");
}
template <int N> DI void glds(unsigned voff, const char* base, unsigned lds) {
  glds1<0>(voff, base, lds);
  if constexpr (N > 1) glds1<1024>(voff, base, lds);
  if constexpr (N > 2) glds1<2048>(voff, base, lds);
  if constexpr (N > 3) glds1<3072>(voff, base, lds);
}
template <int N> DI void wait2(u4& a, u4& b) { asm volatile("s_waitcnt vmcnt(%2)" : "+v"(a), "+v"(b) : "i"(N) : "memory"); }
template <int N> DI void waitv() { asm volatile("s_waitcnt vmcnt(%0)" :: "i"(N) : "memory"); }
constexpr int g1_ops(int s, int G) { return s > 16 ? 0 : (s == 16 ? G : G + 2 + ((WARM2 && G == 3 && s >= 3) ? 1 : 0)); }
constexpr int g1_allow(int s, int G) {
  int n = 0;
  for (int i = s + 1; i < (s == 0 ? G1_AHEAD : s + G1_AHEAD - 1); ++i) n += g1_ops(i, G);
  return n;
}
template <int S, int G> DI void g1_wait(u4 (&act)[32]) {
  if constexpr (S < 16) wait2<g1_allow(S, G)>(act[2 * S], act[2 * S + 1]);
  else waitv<0>();
  __builtin_amdgcn_s_barrier();
  asm volatile("" ::: "memory");
}
struct G1Ctx { unsigned voff, lds; const char *wbase, *abase, *warm, *sl; };
template <int NT, int S, int J> DI void g1_issue(const G1Ctx& cx, u4 (&act)[32]) {
  constexpr int G = NT / 2;
  if constexpr (J < g1_ops(S, G)) {
    if constexpr (J < G) glds1<J * 1024>(cx.voff, cx.wbase + S * G1_SLOT, cx.lds + (S % G1_NSLOT) * G1_SLOT);
    else if constexpr (J < G + 2) ld1(act[2 * S + J - G], cx.voff, cx.abase + (2 * S + J - G) * 1024);
    else glds1<0>(cx.voff, cx.warm + (S - 3) * 4096, cx.lds + G1_WARM);
  }
}
template <int NT, int S> DI void g1_issue_all(const G1Ctx& cx, u4 (&act)[32]) {
  g1_issue<NT, S, 0>(cx, act); g1_issue<NT, S, 1>(cx, act); g1_issue<NT, S, 2>(cx, act);
  g1_issue<NT, S, 3>(cx, act); g1_issue<NT, S, 4>(cx, act); g1_issue<NT, S, 5>(cx, act);
}
template <int NT, int I, int N> DI void g1_unit(f16v (&acc)[NT], u4 (&act)[32], u4 (&fr)[2][NT], const u4& bx, const G1Ctx& cx) {
  constexpr int NX = I + 1, RS = NX / 2 + G1_AHEAD - 1;
  if constexpr (N < NT) {
    if constexpr (NX < 33) fr[NX & 1][N] = *(const u4*)(cx.sl + ((NX / 2) % G1_NSLOT) * G1_SLOT + (NX & 1) * 6144 + N * 1024);
    if constexpr (NX >= 2) {
      if constexpr (NT == 6) { if constexpr ((N & 1) == 0) g1_issue<NT, RS, (NX & 1) * 3 + N / 2>(cx, act); }
      else { g1_issue<NT, RS, (NX & 1) * 2 + N>(cx, act); }
    }
    acc[N] = mm32b(asB(fr[I & 1][N]), asB(I < 32 ? act[I < 32 ? I : 0] : bx), acc[N]);
    __builtin_amdgcn_sched_barrier(0);
    g1_unit<NT, I, N + 1>(acc, act, fr, bx, cx);
  }
}
template <int NT, int I, class Stats>
DI void g1_step(f16v (&acc)[NT], u4 (&act)[32], u4 (&fr)[2][NT], u4& bx, const G1Ctx& cx, Stats& stats STAMP_ARG) {
  constexpr int NX = I + 1;
  if constexpr (NX < 33 && (NX & 1) == 0) {
    g1_wait<NX / 2, NT / 2>(act);
    if constexpr (NX / 2 == 7) { stats(); __builtin_amdgcn_sched_barrier(0); }
    if constexpr (NX / 2 == 1) STAMP(3);
    if constexpr (NX / 2 == 4) STAMP(4);
    if constexpr (NX / 2 == 8) STAMP(5);
    if constexpr (NX / 2 == 12) STAMP(6);
    if constexpr (NX / 2 == 16) STAMP(7);
  }
  g1_unit<NT, I, 0>(acc, act, fr, bx, cx);
  if constexpr (NX < 33) g1_step<NT, NX>(acc, act, fr, bx, cx, stats STAMP_PASS);
}

#define WT_H WT
template <int NT> DI void g1_body(char* ws, char* smem, int s2, int chunk, int b, int w, int l STAMP_ARG) {
  const int wo = (NT == 6) ? w * 3072 : (w >> 1) * 6144 + (w & 1) * 1024;
  G1Ctx cx;
  cx.voff = l * 16;
  cx.lds = (unsigned)(size_t)(LDS3 char*)smem + wo;
  cx.abase = ws + OFF_X1 + (size_t)(s2 * 4 + w) * 32768;
  cx.wbase = ws + OFF_W1 + (size_t)chunk * 33 * 6144 + wo;
  cx.warm = ws + OFF_W2 + (size_t)(b >> 3) * 53248 + w * 1024;
  cx.sl = smem + l * 16;
  u2 stv[8];
  u4 act[32];
  {
    const char* sb = ws + OFF_ST + (size_t)s2 * 16384 + w * 256;
    const unsigned so = (l & 31) * 8 + (l >> 5) * 8192;
    asm volatile("s_nop 4\n\tglobal_load_dwordx2 %0, %8, %9\n\tglobal_load_dwordx2 %1, %8, %9 offset:1024\n\t"
                 "global_load_dwordx2 %2, %8, %9 offset:2048\n\tglobal_load_dwordx2 %3, %8, %9 offset:3072\n\t"
                 "global_load_dwordx2 %4, %8, %10\n\tglobal_load_dwordx2 %5, %8, %10 offset:1024\n\t"
                 "global_load_dwordx2 %6, %8, %10 offset:2048\n\tglobal_load_dwordx2 %7, %8, %10 offset:3072"
                 : "=&v"(stv[0]), "=&v"(stv[1]), "=&v"(stv[2]), "=&v"(stv[3]), "=&v"(stv[4]), "=&v"(stv[5]), "=&v"(stv[6]), "=&v"(stv[7])
                 : "v"(so), "s"(sb), "s"(sb + 4096) : "memory");
  }
  g1_issue_all<NT, 0>(cx, act);
  g1_issue_all<NT, 1>(cx, act);
  g1_issue_all<NT, 2>(cx, act);
  g1_issue_all<NT, 3>(cx, act);
  STAMP(1);

  f16v acc[NT];
#pragma unroll
  for (int n = 0; n < NT; ++n)
#pragma unroll
    for (int e = 0; e < 16; ++e) acc[n][e] = 0.f;
  float sd = 0.f;
  u4 bx;
  auto stats = [&]() {
    asm volatile("" : "+v"(stv[0]), "+v"(stv[1]), "+v"(stv[2]), "+v"(stv[3]), "+v"(stv[4]), "+v"(stv[5]), "+v"(stv[6]), "+v"(stv[7]));
    f2 sv = {0.f, 0.f};
#pragma unroll
    for (int i = 0; i < 8; ++i) sv += __builtin_bit_cast(f2, stv[i]);
    const float sm = sv[0] + __shfl_xor(sv[0], 32), sq = sv[1] + __shfl_xor(sv[1], 32);
    const float mean = sm * (1.f / 512.f);
    const float var = fmaxf(sq * (1.f / 512.f) - mean * mean, 0.f);
    sd = rbf(__builtin_amdgcn_sqrtf(rbf(rbf(var) + 1e-5f)));
    const float e = (l >> 5) ? sd : -mean;
    const unsigned e2 = pkbf(e, e);
    bx = u4{e2, e2, e2, e2};
  };
  u4 fr[2][NT];
  g1_wait<0, NT / 2>(act);
  STAMP(2);
#pragma unroll
  for (int n = 0; n < NT; ++n) fr[0][n] = *(const u4*)(cx.sl + n * 1024);
  __builtin_amdgcn_sched_barrier(0);
  g1_step<NT, 0>(acc, act, fr, bx, cx, stats STAMP_PASS);
  STAMP(8);
  const float rs = __builtin_amdgcn_rcpf(sd), rs2 = rs * rs;
  const unsigned hb = OFF_H + ((s2 * 4 + w) * 86 + chunk * 6) * 1024 + l * 16;
#pragma unroll
  for (int n = 0; n < NT; ++n) {
    u4 o;
#pragma unroll
    for (int e = 0; e < 4; ++e) {
      const int i0 = 8 * (e >> 1) + 2 * (e & 1);
      o[e] = pkbf((acc[n][i0] * rs2) * acc[n][i0 + 4], (acc[n][i0 + 1] * rs2) * acc[n][i0 + 5]);
    }
    st16<WT_H>(ws, hb + n * 1024, o);
  }
}

__global__ __launch_bounds__(256, 2) void kG1(char* ws) {
  extern __shared__ __attribute__((aligned(16))) char smem[];
  const int b = blockIdx.x, tid = threadIdx.x;
  const int w = __builtin_amdgcn_readfirstlane(tid >> 6), l = tid & 63;
  STAMP_DECL;
  STAMP(0);
  STAMPK;
  const int s2 = (b & 7) * 2 + ((b >> 3) & 1), chunk = b >> 4;
  if (chunk < 14) g1_body<6>(ws, smem, s2, chunk, b, w, l STAMP_PASS);
  else g1_body<2>(ws, smem, s2, chunk, b, w, l STAMP_PASS);
  STAMPEND(3);
}

constexpr int G2_RS = 68;
constexpr int G2_SLOT = 32768, G2_NSLOT = 5, G2_LDS = G2_SLOT * G2_NSLOT;
#define G2_AHEAD 3
constexpr int g2_allow(int s) { return 4 * ((s + G2_AHEAD - 1 < 10 ? s + G2_AHEAD - 1 : 10) - s); }

DI int g2_rot(int s, int rot) { const int v = s + rot; return v >= 11 ? v - 11 : v; }
template <int S> struct G2Step {
  static DI void run(f16v& acc, const char* src, int rot, unsigned voff, unsigned dst, const char* rh, const char* rw STAMP_ARG) {
    waitv<g2_allow(S)>();
    __builtin_amdgcn_s_barrier();
    asm volatile("" ::: "memory");
    if constexpr (S + G2_AHEAD <= 10) glds<4>(voff, src + g2_rot(S + G2_AHEAD, rot) * 4096, dst + ((S + G2_AHEAD) % G2_NSLOT) * G2_SLOT);
    if constexpr (S == 0) STAMP(2);
    if constexpr (S == 5) STAMP(3);
    if constexpr (S == 10) STAMP(4);
    u4 hf[4], wf[4];
#pragma unroll
    for (int k = 0; k < 4; ++k) {
      hf[k] = *(const u4*)(rh + (S % G2_NSLOT) * G2_SLOT + k * 1024);
      wf[k] = *(const u4*)(rw + (S % G2_NSLOT) * G2_SLOT + k * 1024);
    }
#pragma unroll
    for (int k = 0; k < 3; ++k) acc = mm32b(asB(wf[k]), asB(hf[k]), acc);
    if (g2_rot(S, rot) != 10) acc = mm32b(asB(wf[3]), asB(hf[3]), acc);
    __builtin_amdgcn_sched_barrier(0);
    if constexpr (S < 10) G2Step<S + 1>::run(acc, src, rot, voff, dst, rh, rw STAMP_PASS);
  }
};

__global__ __launch_bounds__(512, 2) void kG2(char* ws, u16* __restrict__ out) {
  extern __shared__ __attribute__((aligned(16))) char smem[];
  const int b = blockIdx.x, tid = threadIdx.x;
  const int w = __builtin_amdgcn_readfirstlane(tid >> 6), l = tid & 63;
  STAMP_DECL;
  STAMP(0);
  STAMPK;
  const int rt = (b & 7) * 4 + ((b >> 3) & 3), ct = b >> 5;
  const int kh = w >> 2, a = (w >> 1) & 1, c = w & 1;
#define ROT2 0
  const int rot = ROT2 ? (ct + 3 * (rt & 3)) % 11 : 0;
  const unsigned voff = l * 16;
  const char* src = ws + ((w & 2) ? OFF_W2 + (size_t)(2 * ct + c) * 86 * 1024 : OFF_H + (size_t)(2 * rt + c) * 86 * 1024) + kh * 43 * 1024;
  const unsigned dst = (unsigned)(size_t)(LDS3 char*)smem + (w & 3) * 8192 + kh * 4096;
  const int row = tid >> 3, oct = tid & 7;
  const int dcol = 64 * ct + 8 * oct;
  u4 xr, gb0, gb1;
  {
    const unsigned xo = OFF_X1 + ((2 * rt + (row >> 5)) * 32 + (dcol >> 4)) * 1024 + ((row & 31) + 32 * (oct & 1)) * 16;
    const unsigned go = OFF_MOD + (2560 + dcol) * 4;
    asm volatile("s_nop 4\n\tglobal_load_dwordx4 %0, %3, %5\n\tglobal_load_dwordx4 %1, %4, %5\n\tglobal_load_dwordx4 %2, %4, %5 offset:16"
                 : "=&v"(xr), "=&v"(gb0), "=&v"(gb1) : "v"(xo), "v"(go), "s"(ws) : "memory");
  }
#pragma unroll
  for (int s = 0; s < G2_AHEAD; ++s) {
    glds<4>(voff, src + g2_rot(s, rot) * 4096, dst + s * G2_SLOT);
    if (s + 1 < G2_AHEAD) __builtin_amdgcn_s_barrier();
  }
  STAMP(1);
  f16v acc;
#pragma unroll
  for (int e = 0; e < 16; ++e) acc[e] = 0.f;
  G2Step<0>::run(acc, src, rot, voff, dst, smem + a * 8192 + kh * 4096 + l * 16, smem + (2 + c) * 8192 + kh * 4096 + l * 16 STAMP_PASS);
  float* red = (float*)(smem + 3 * G2_SLOT);
#pragma unroll
  for (int qd = 0; qd < 4; ++qd)
    *(f4*)(red + (kh * 64 + 32 * a + (l & 31)) * G2_RS + 32 * c + 8 * qd + 4 * (l >> 5)) =
        f4{acc[4 * qd], acc[4 * qd + 1], acc[4 * qd + 2], acc[4 * qd + 3]};
  asm volatile("s_waitcnt lgkmcnt(0)" : "+v"(xr), "+v"(gb0), "+v"(gb1) :: "memory");
  __builtin_amdgcn_s_barrier();
  asm volatile("" ::: "memory");
  STAMP(5);
  {
    const float* p = red + row * G2_RS + 8 * oct;
    const f4 s0 = *(const f4*)p + *(const f4*)(p + 64 * G2_RS), s1 = *(const f4*)(p + 4) + *(const f4*)(p + 64 * G2_RS + 4);
    const f4 g0 = __builtin_bit_cast(f4, gb0), g1 = __builtin_bit_cast(f4, gb1);
    const u4 o = {pkbf(fmaf(s0[0], g0[0], bflo(xr[0])), fmaf(s0[1], g0[1], bfhi(xr[0]))),
                  pkbf(fmaf(s0[2], g0[2], bflo(xr[1])), fmaf(s0[3], g0[3], bfhi(xr[1]))),
                  pkbf(fmaf(s1[0], g1[0], bflo(xr[2])), fmaf(s1[1], g1[1], bfhi(xr[2]))),
                  pkbf(fmaf(s1[2], g1[2], bflo(xr[3])), fmaf(s1[3], g1[3], bfhi(xr[3])))};
    const int rp = 64 * rt + row;
    const int s = 16 * (rp & 127) + (rp >> 7);
    *(u4*)(out + (size_t)s * 512 + dcol) = o;
  }
  STAMPEND(4);
}

extern "C" void kernel_launch(void* const* d_in, const int* in_sizes, int n_in, void* d_out, int out_size,
                              void* d_ws, size_t ws_size, hipStream_t stream) {
  const u16* x = (const u16*)d_in[0];
  const u16* wr = (const u16*)d_in[1];
  const u16* wi = (const u16*)d_in[2];
  const u16* wg = (const u16*)d_in[4];
  const u16* wu = (const u16*)d_in[5];
  const u16* wd = (const u16*)d_in[6];
  const u16* cv = (const u16*)d_in[7];
  const u16* wm = (const u16*)d_in[8];
  char* ws = (char*)d_ws;
  (void)hipFuncSetAttribute((const void*)kB, hipFuncAttributeMaxDynamicSharedMemorySize, KB_LDS);
  (void)hipFuncSetAttribute((const void*)kG1, hipFuncAttributeMaxDynamicSharedMemorySize, G1_LDS);
  (void)hipFuncSetAttribute((const void*)kG2, hipFuncAttributeMaxDynamicSharedMemorySize, G2_LDS);
  kA<<<KA_MAIN + KA_GEMV + KA_WPREP, 512, 0, stream>>>(x, wr, wi, cv, wm, ws);
  kB<<<KB_MAIN + KB_W1 + KB_GEMV + KB_W2, 512, KB_LDS, stream>>>(wg, wu, wd, cv, wm, ws);
  kC<<<256, 512, 0, stream>>>(x, ws);
  kG1<<<240, 256, G1_LDS, stream>>>(ws);
  kG2<<<256, 512, G2_LDS, stream>>>(ws, (u16*)d_out);
}
```

```cpp
#include <hip/hip_runtime.h>
#include <hip/hip_bf16.h>
#include <stdint.h>

typedef unsigned short u16;
typedef unsigned long long u64;
typedef _Float16 h2 __attribute__((ext_vector_type(2)));
typedef _Float16 h4 __attribute__((ext_vector_type(4)));
typedef _Float16 h8 __attribute__((ext_vector_type(8)));
typedef __bf16 b2 __attribute__((ext_vector_type(2)));
typedef __bf16 b8 __attribute__((ext_vector_type(8)));
typedef float f2 __attribute__((ext_vector_type(2)));
typedef float f4 __attribute__((ext_vector_type(4)));
typedef float f16v __attribute__((ext_vector_type(16)));
typedef unsigned u2 __attribute__((ext_vector_type(2)));
typedef unsigned u4 __attribute__((ext_vector_type(4)));
typedef unsigned u4a __attribute__((ext_vector_type(4), aligned(4)));
typedef short s4 __attribute__((ext_vector_type(4)));
typedef __attribute__((address_space(1))) unsigned gu32;
typedef __attribute__((address_space(1))) u64 gu64;
#define DI __device__ __forceinline__
#define LDS3 __attribute__((address_space(3)))
#define RLX __ATOMIC_RELAXED, __HIP_MEMORY_SCOPE_AGENT

constexpr int FH = 1376;
constexpr unsigned OFF_MOD = 0;
constexpr unsigned OFF_MMAX = 12288;
constexpr unsigned OFF_YSC = 14336;
constexpr unsigned OFF_EPOCH = 20480;
constexpr unsigned OFF_ALIVE = OFF_EPOCH + 1024;
constexpr unsigned OFF_BETA = OFF_ALIVE + 1024;
#define FSTR 64
constexpr unsigned FS = FSTR;
constexpr unsigned OFF_FA = OFF_BETA + 1024;
constexpr unsigned OFF_FM = OFF_FA + 128 * FS;
constexpr unsigned OFF_FQ = OFF_FM + 64 * FS, OFF_FB = OFF_FQ + 48 * FS, OFF_FC = OFF_FB + 72 * FS;
constexpr unsigned OFF_FD = OFF_FC + 128 * FS;
constexpr unsigned OFF_PQ = 229376;
constexpr unsigned OFF_Y = 262144;
constexpr unsigned OFF_V = OFF_Y + 2097152;
constexpr unsigned OFF_X1 = OFF_V + 2097152;
constexpr unsigned OFF_XS = OFF_X1 + 2097152;
constexpr unsigned OFF_ST = OFF_XS + 2097152;
constexpr unsigned OFF_H = OFF_ST + 262144;
static_assert(OFF_FD + 256 * FS <= OFF_PQ && OFF_PQ + 88 * 256 <= OFF_Y, "workspace overlap");

#define STAMP(idx)

DI float bf2f(unsigned v) { return __builtin_bit_cast(float, v << 16); }
DI float bflo(unsigned v) { return __builtin_bit_cast(float, v << 16); }
DI float bfhi(unsigned v) { return __builtin_bit_cast(float, v & 0xffff0000u); }
DI unsigned pkbf(float a, float b) { f2 v = {a, b}; return __builtin_bit_cast(unsigned, __builtin_convertvector(v, b2)); }
DI unsigned pkh(float a, float b) { f2 v = {a, b}; return __builtin_bit_cast(unsigned, __builtin_convertvector(v, h2)); }
DI float rbf(float a) { return bflo(pkbf(a, a)); }
DI float cosr(float rev) { return __builtin_amdgcn_cosf(rev); }
DI float sinr(float rev) { return __builtin_amdgcn_sinf(rev); }
DI b2 asB2(unsigned v) { return __builtin_bit_cast(b2, v); }
DI unsigned asU(float v) { return __builtin_bit_cast(unsigned, v); }
DI float asF(unsigned v) { return __builtin_bit_cast(float, v); }
DI unsigned bfi(unsigned m, unsigned a, unsigned b) { return (m & a) | (~m & b); }
DI h8 asH(u4 v) { return __builtin_bit_cast(h8, v); }
DI b8 asB(u4 v) { return __builtin_bit_cast(b8, v); }
DI f4 mm16(h8 a, h8 b, f4 c) { return __builtin_amdgcn_mfma_f32_16x16x32_f16(a, b, c, 0, 0, 0); }
DI f16v mm32h(h8 a, h8 b, f16v c) { return __builtin_amdgcn_mfma_f32_32x32x16_f16(a, b, c, 0, 0, 0); }
DI f16v mm32b(b8 a, b8 b, f16v c) { return __builtin_amdgcn_mfma_f32_32x32x16_bf16(a, b, c, 0, 0, 0); }
DI f16v zero16() { f16v z; _Pragma("unroll") for (int e = 0; e < 16; ++e) z[e] = 0.f; return z; }
DI h8 pack8(f4 r, f4 i) { u4 v = {pkh(r[0], r[1]), pkh(r[2], r[3]), pkh(i[0], i[1]), pkh(i[2], i[3])}; return asH(v); }
template <int CTRL> DI float dppadd(float v) { return v + __builtin_amdgcn_update_dpp(0.f, v, CTRL, 0xf, 0xf, false); }
template <int CTRL> DI unsigned dppmax(unsigned v) { return max(v, (unsigned)__builtin_amdgcn_update_dpp(0, (int)v, CTRL, 0xf, 0xf, false)); }
DI unsigned wavemax_bits(unsigned v) {
  v = dppmax<0x128>(v); v = dppmax<0x124>(v); v = dppmax<0x122>(v); v = dppmax<0x121>(v);
  return max(max((unsigned)__builtin_amdgcn_readlane(v, 0), (unsigned)__builtin_amdgcn_readlane(v, 16)),
             max((unsigned)__builtin_amdgcn_readlane(v, 32), (unsigned)__builtin_amdgcn_readlane(v, 48)));
}
DI unsigned scale_exp(unsigned bits) { return min(max((bits >> 23) & 0xffu, 1u), 252u); }
DI float scale_up(unsigned e) { return asF((e + 1) << 23); }
DI float scale_inv(unsigned e) { return asF((253 - e) << 23); }
DI f2 lo2(f4 v) { return __builtin_shufflevector(v, v, 0, 1); }
DI f2 hi2(f4 v) { return __builtin_shufflevector(v, v, 2, 3); }
DI h8 frag4(unsigned a, unsigned b, unsigned c, unsigned d) { u4 v = {a, b, c, d}; return asH(v); }
DI s4 trread(const char* p) { return __builtin_amdgcn_ds_read_tr16_b64_v4i16((LDS3 s4*)p); }
DI h8 trfrag(const char* p0, const char* p1) {
  const u2 lo = __builtin_bit_cast(u2, trread(p0)), hi = __builtin_bit_cast(u2, trread(p1));
  return frag4(lo[0], lo[1], hi[0], hi[1]);
}
constexpr unsigned NEG2 = 0x80008000u;
DI void cs4(float r0, float step, float sc, unsigned& c01, unsigned& c23, unsigned& s01, unsigned& s23) {
  float c[4], s[4];
#pragma unroll
  for (int i = 0; i < 4; ++i) {
    const float r = __builtin_amdgcn_fractf(r0 + step * i);
    c[i] = sc * cosr(r);
    s[i] = sc * sinr(r);
  }
  c01 = pkh(c[0], c[1]); c23 = pkh(c[2], c[3]); s01 = pkh(s[0], s[1]); s23 = pkh(s[2], s[3]);
}

DI __amdgpu_buffer_rsrc_t wsr(char* ws) { return __builtin_amdgcn_make_buffer_rsrc(ws, 0, 0x4000000, 0x00020000); }
DI void st16(char* ws, unsigned off, u4 v) { __builtin_amdgcn_raw_buffer_store_b128(v, wsr(ws), off, 0, 16); }
DI void st8(char* ws, unsigned off, u2 v) { __builtin_amdgcn_raw_buffer_store_b64(v, wsr(ws), off, 0, 16); }
DI void st4(char* ws, unsigned off, unsigned v) { __builtin_amdgcn_raw_buffer_store_b32(v, wsr(ws), off, 0, 16); }
DI u4 ld16(char* ws, unsigned off) { return __builtin_amdgcn_raw_buffer_load_b128(wsr(ws), off, 0, 16); }
DI u2 ld8(char* ws, unsigned off) { return __builtin_amdgcn_raw_buffer_load_b64(wsr(ws), off, 0, 16); }
DI unsigned ld4(char* ws, unsigned off) { return __builtin_amdgcn_raw_buffer_load_b32(wsr(ws), off, 0, 16); }
template <int N> DI void waitv() { asm volatile("s_waitcnt vmcnt(%0)" :: "i"(N) : "memory"); }
DI void bar() { asm volatile("s_waitcnt lgkmcnt(0)" ::: "memory"); __builtin_amdgcn_s_barrier(); asm volatile("" ::: "memory"); }
constexpr int SPIN_CAP = 1 << 16;
DI void publish(char* ws, unsigned flag_off, unsigned tag) {
  waitv<0>();
  bar();
  if (threadIdx.x == 0) __hip_atomic_store((gu32*)(ws + flag_off), tag, RLX);
}
template <unsigned STRIDE, int NAP = 1> DI void poll(char* ws, unsigned off, int n, unsigned tag, int l) {
  gu32* p0 = (gu32*)(ws + off + min(l, n - 1) * STRIDE);
  gu32* p1 = (gu32*)(ws + off + min(l + 64, n - 1) * STRIDE);
  for (int spin = 0; spin < SPIN_CAP; ++spin) {
    bool ok = __hip_atomic_load(p0, RLX) == tag;
    if (n > 64) ok &= __hip_atomic_load(p1, RLX) == tag;
    if (__all(ok)) break;
    __builtin_amdgcn_s_sleep(NAP);
  }
  __builtin_amdgcn_fence(__ATOMIC_ACQUIRE, "wavefront");
}
DI void await(char* ws, unsigned off, int n, unsigned tag, int w, int l) {
  if (w == 0) poll<FS>(ws, off, n, tag, l);
  bar();
}

constexpr int P0_RPL = 16 * 136;
constexpr int P0_RAW = 8 * P0_RPL + 4 * (P0_RPL + 32);
constexpr int P0_STAT = P0_RAW + 128 * 128;

DI void p0_dft(u4 (&bfr)[8], int m, int h) {
  const int slot = m >> 1, keff = (m == 1) ? 64 : slot;
  const float st = (float)keff * (1.f / 128.f), ph = ((m & 1) && slot) ? 0.25f : 0.f;
#pragma unroll
  for (int k0 = 0; k0 < 2; ++k0) {
    const float r0 = st * (16 * k0 + 8 * h) + ph;
    u4 cc, ss;
    unsigned a0, a1, a2, a3;
    cs4(r0, st, 0.08838834764831845f, a0, a1, a2, a3);
    cc[0] = a0; cc[1] = a1; ss[0] = a2; ss[1] = a3;
    cs4(r0 + 4.f * st, st, 0.08838834764831845f, a0, a1, a2, a3);
    cc[2] = a0; cc[3] = a1; ss[2] = a2; ss[3] = a3;
    bfr[k0] = cc;
#pragma unroll
    for (int kq = 1; kq < 4; ++kq) {
      const int q = (keff * kq) & 3;
      const unsigned sg = ((q + 1) & 2) ? NEG2 : 0u;
#pragma unroll
      for (int e = 0; e < 4; ++e) bfr[2 * kq + k0][e] = ((q & 1) ? ss[e] : cc[e]) ^ sg;
    }
  }
  asm volatile("" : "+v"(bfr[0]), "+v"(bfr[1]), "+v"(bfr[2]), "+v"(bfr[3]), "+v"(bfr[4]), "+v"(bfr[5]), "+v"(bfr[6]), "+v"(bfr[7]));
}

DI void phase0(const u16* __restrict__ x, char* ws, char* smem, int s2, int c, int w, int l) {
  const int i16 = l & 15, g = l >> 4, n32 = l & 31, h = l >> 5;
  const int rg = (w + c) & 7;
  const int r4 = (l >> 2) & 3, pc = l & 3;
  u4 bfr[8], xv[4][4];
  if (w >= 4) p0_dft(bfr, 32 * (w & 3) + n32, h);
  __builtin_amdgcn_sched_barrier(0);
  {
    const u16* xr = x + (size_t)(16 * (16 * rg + r4) + s2) * 512 + 32 * g + 8 * pc;
#pragma unroll
    for (int G = 0; G < 4; ++G)
#pragma unroll
      for (int q = 0; q < 4; ++q) xv[G][q] = *(const u4*)(xr + G * (64 * 512) + 128 * ((q + w) & 3));
  }
  __builtin_amdgcn_sched_barrier(0);
  if (w < 4) p0_dft(bfr, 32 * (w & 3) + n32, h);
  unsigned mh = (c & 1) ? ~0u : 0u;
  asm volatile("" : "+s"(mh));
  __builtin_amdgcn_sched_barrier(0);

  char* raw = smem + w * P0_RPL;
  char* rawp = ((pc == (c >> 1)) ? raw : smem + 8 * P0_RPL + pc * (P0_RPL + 32)) + r4 * 136 + g * 8;
  float* stat = (float*)(smem + P0_STAT) + w * 32;
  const u4 ones = {0x3f803f80u, 0x3f803f80u, 0x3f803f80u, 0x3f803f80u};
  f4 as[4], aq[4];
#pragma unroll
  for (int G = 0; G < 4; ++G) {
    as[G] = f4{0.f, 0.f, 0.f, 0.f};
    aq[G] = f4{0.f, 0.f, 0.f, 0.f};
#pragma unroll
    for (int q = 0; q < 4; ++q) {
      as[G] = __builtin_amdgcn_mfma_f32_16x16x32_bf16(asB(xv[G][q]), asB(ones), as[G], 0, 0, 0);
      aq[G] = __builtin_amdgcn_mfma_f32_16x16x32_bf16(asB(xv[G][q]), asB(xv[G][q]), aq[G], 0, 0, 0);
      *(u2*)(rawp + G * (4 * 136) + ((q + w) & 3) * 32) = u2{bfi(mh, xv[G][q][2], xv[G][q][0]), bfi(mh, xv[G][q][3], xv[G][q][1])};
      __builtin_amdgcn_sched_barrier(0);
    }
  }
  const int row = l >> 2;
  u2 rv[4];
#pragma unroll
  for (int i = 0; i < 4; ++i) rv[i] = *(const u2*)(raw + row * 136 + pc * 32 + i * 8);
  {
    float dq[4], ds[4];
#pragma unroll
    for (int G = 0; G < 4; ++G) {
      dq[G] = (pc & 2) ? ((pc & 1) ? aq[G][3] : aq[G][2]) : ((pc & 1) ? aq[G][1] : aq[G][0]);
      ds[G] = (as[G][0] + as[G][1]) + (as[G][2] + as[G][3]);
    }
#pragma unroll
    for (int G = 0; G < 4; ++G) dq[G] = dppadd<0xB1>(dq[G]);
#pragma unroll
    for (int G = 0; G < 4; ++G) dq[G] = dppadd<0x4E>(dq[G]);
    const float q1 = (pc & 2) ? ((pc & 1) ? dq[3] : dq[2]) : ((pc & 1) ? dq[1] : dq[0]);
    const float s1 = (pc & 2) ? ((pc & 1) ? ds[3] : ds[2]) : ((pc & 1) ? ds[1] : ds[0]);
    if (r4 == g) *(f2*)(stat + (4 * pc + g) * 2) = f2{s1, q1};
  }
  STAMP(1);
  const f2 sv = *(const f2*)(stat + row * 2);
  const float mean = sv[0] * (1.f / 512.f);
  const float var = fmaxf(sv[1] * (1.f / 512.f) - mean * mean, 0.f);
  const float sd = rbf(__builtin_amdgcn_sqrtf(rbf(rbf(var) + 1e-5f)));
  const float rs = __builtin_amdgcn_rcpf(sd);
  const float nb = -rbf(mean) * rs;
  char* nimg = smem + P0_RAW;
  {
    const int s1 = 16 * rg + row;
    char* nb_ = nimg + s1 * 128 + pc * 32;
    const int sw = s1 & 2;
#pragma unroll
    for (int tp = 0; tp < 2; ++tp) {
      u4 o;
#pragma unroll
      for (int t = 0; t < 2; ++t) {
        float v[4];
#pragma unroll
        for (int i = 0; i < 4; ++i) v[i] = fmaf(t ? bfhi(rv[i][tp]) : bflo(rv[i][tp]), rs, nb);
        o[2 * t] = pkh(v[0], v[1]);
        o[2 * t + 1] = pkh(v[2], v[3]);
      }
      *(u4*)(nb_ + (((2 * tp) ^ sw) * 8)) = o;
    }
  }
  bar();
  STAMP(2);
  {
    const int q = i16 >> 2, p4 = i16 & 3, tp = w >> 2;
    const char* a = nimg + (8 * h + q) * 128 + p4 * 32 + (((2 * tp + (g & 1)) ^ (q & 2)) * 8);
    f16v acc = zero16();
#pragma unroll
    for (int ks = 0; ks < 8; ++ks) acc = mm32h(trfrag(a + ks * 2048, a + ks * 2048 + 512), asH(bfr[ks]), acc);
    char* yb = raw + (n32 >> 1) * 136 + (n32 & 1) * 32 + h * 8;
#pragma unroll
    for (int qd = 0; qd < 4; ++qd)
      *(u2*)(yb + (qd >> 1) * 64 + (qd & 1) * 16) = u2{pkh(acc[4 * qd], acc[4 * qd + 1]), pkh(acc[4 * qd + 2], acc[4 * qd + 3])};
#pragma unroll
    for (int i = 0; i < 2; ++i) {
      const int sl = 8 * i + (l >> 3);
      const char* p = raw + sl * 136 + (l & 7) * 16;
      const u2 lo = *(const u2*)p, hi = *(const u2*)(p + 8);
      st16(ws, OFF_Y + (16 * (w & 3) + sl) * 32768 + s2 * 2048 + c * 256 + tp * 128 + (l & 7) * 16, u4{lo[0], lo[1], hi[0], hi[1]});
    }
  }
}

template <int NC> DI void mod_gemv(const u16* __restrict__ cvec, const u16* __restrict__ wmod, char* ws, char* smem, int col0, int tid) {
  constexpr int OC = NC / 8, RG = 512 / OC, NR = OC, RSF = NC + 4;
  const int oct = tid % OC, rg = tid / OC;
  u4 wv[NR];
  u16 cv[NR];
#pragma unroll
  for (int i = 0; i < NR; ++i) {
    cv[i] = cvec[rg + RG * i];
    wv[i] = *(const u4*)(wmod + (size_t)(rg + RG * i) * 3072 + col0 + 8 * oct);
  }
  float acc[8];
#pragma unroll
  for (int e = 0; e < 8; ++e) acc[e] = 0.f;
#pragma unroll
  for (int i = 0; i < NR; ++i) {
    const float cf = bf2f(cv[i]);
    const float sl = rbf(cf / (1.f + __expf(-cf)));
#pragma unroll
    for (int e = 0; e < 4; ++e) {
      acc[2 * e] = fmaf(sl, bflo(wv[i][e]), acc[2 * e]);
      acc[2 * e + 1] = fmaf(sl, bfhi(wv[i][e]), acc[2 * e + 1]);
    }
  }
  float* red = (float*)smem;
  *(f4*)(red + rg * RSF + 8 * oct) = f4{acc[0], acc[1], acc[2], acc[3]};
  *(f4*)(red + rg * RSF + 8 * oct + 4) = f4{acc[4], acc[5], acc[6], acc[7]};
  bar();
  constexpr int PARTS = 512 / NC, PER = RG / PARTS;
  float v = 0.f;
#pragma unroll
  for (int i = 0; i < PER; ++i) v += red[(PER * (tid / NC) + i) * RSF + tid % NC];
  float* red2 = red + RG * RSF;
  red2[tid] = v;
  bar();
  if (tid < 64) {
    const int cidx = tid % NC;
    v = 0.f;
#pragma unroll
    for (int i = 0; i < PARTS; ++i) v += red2[i * NC + cidx];
    v = rbf(v);
    const int sec = col0 >> 9;
    if (sec == 1 || sec == 4) v = rbf(1.f + v);
    if (tid < NC) st4(ws, OFF_MOD + (col0 + cidx) * 4, asU(v));
    if (NC == 32) {
      const unsigned mx = wavemax_bits(asU(v) & 0x7fffffffu);
      if (tid == 0) st4(ws, OFF_MMAX + (col0 >> 5) * 4, mx);
    }
  }
}

DI void pq_job(const u16* __restrict__ wg, const u16* __restrict__ wu, char* ws, char* smem, unsigned tag, int j, int w, int l) {
  const int tid = w * 64 + l, oct = tid & 7, rg = tid >> 3;
  const u16* src = ((oct & 4) ? wu : wg) + 32 * j + 8 * (oct & 3);
  u4 wv[8];
#pragma unroll
  for (int i = 0; i < 8; ++i) wv[i] = *(const u4*)(src + (size_t)(rg + 64 * i) * FH);
  await(ws, OFF_FM + 32 * FS, 16, tag, w, l);
  float pa[8], qa[8];
#pragma unroll
  for (int e = 0; e < 8; ++e) { pa[e] = 0.f; qa[e] = 0.f; }
#pragma unroll
  for (int i = 0; i < 8; ++i) {
    const float cb = asF(ld4(ws, OFF_MOD + (2048 + rg + 64 * i) * 4)), sb = asF(ld4(ws, OFF_MOD + (1536 + rg + 64 * i) * 4));
#pragma unroll
    for (int e = 0; e < 4; ++e) {
      const float v0 = bflo(wv[i][e]), v1 = bfhi(wv[i][e]);
      pa[2 * e] = fmaf(cb, v0, pa[2 * e]); pa[2 * e + 1] = fmaf(cb, v1, pa[2 * e + 1]);
      qa[2 * e] = fmaf(sb, v0, qa[2 * e]); qa[2 * e + 1] = fmaf(sb, v1, qa[2 * e + 1]);
    }
  }
  float* red = (float*)smem;
  *(f4*)(red + rg * 132 + 8 * oct) = f4{pa[0], pa[1], pa[2], pa[3]};
  *(f4*)(red + rg * 132 + 8 * oct + 4) = f4{pa[4], pa[5], pa[6], pa[7]};
  *(f4*)(red + rg * 132 + 64 + 8 * oct) = f4{qa[0], qa[1], qa[2], qa[3]};
  *(f4*)(red + rg * 132 + 68 + 8 * oct) = f4{qa[4], qa[5], qa[6], qa[7]};
  bar();
  float v = 0.f;
#pragma unroll
  for (int i = 0; i < 16; ++i) v += red[(16 * (tid >> 7) + i) * 132 + (tid & 127)];
  float* red2 = red + 64 * 132;
  red2[tid] = v;
  bar();
  if (tid < 128) {
    v = (red2[tid] + red2[128 + tid]) + (red2[256 + tid] + red2[384 + tid]);
    const float hi = rbf(v);
    const int f = tid & 15, mat = (tid >> 5) & 1;
    const int r = (f & 3) + 4 * (f >> 3) + 8 * mat + 16 * ((f >> 2) & 1);
    st4(ws, OFF_PQ + ((2 * j + ((tid >> 4) & 1)) * 32 + r) * 8 + (tid >> 6) * 4, pkbf(hi, v - hi));
  }
}

constexpr int T1_BYTES = 16 * 2064, T2_BYTES = 32768;
constexpr int P1_TAB = T1_BYTES + T2_BYTES, P1_MOD = P1_TAB + 24 * 1024;

DI void cmulc(f2& r, f2& i, float tc, float ts) {
  const f2 a = r * tc + i * ts;
  i = i * tc - r * ts;
  r = a;
}

DI unsigned filter_prep(const u16* __restrict__ wr, const u16* __restrict__ wi, char* smem, int k1, int tid, u4 (&wv)[4]) {
  u4 tv[4];
#pragma unroll
  for (int it = 0; it < 4; ++it) {
    const int i = tid + 512 * it, row = i >> 5, pc = i & 31;
    const int ksA = k1 + 128 * (row & 15);
    const int ks = (row >> 5) ? ((2048 - ksA) & 2047) : ksA;
    tv[it] = *(const u4a*)((const char*)(((row >> 4) & 1) ? wi : wr) + ((ks * 514) & ~3) + pc * 16);
  }
  if (tid < 64) {
    const int row = tid;
    const int ksA = k1 + 128 * (row & 15);
    const int ks = (row >> 5) ? ((2048 - ksA) & 2047) : ksA;
    const char* src = (const char*)(((row >> 4) & 1) ? wi : wr) + ((ks * 514) & ~3) + 512;
    *(unsigned*)(smem + row * 528 + 512) = *(const unsigned*)src;
  }
#pragma unroll
  for (int it = 0; it < 4; ++it) {
    const int i = tid + 512 * it;
    *(u4*)(smem + (i >> 5) * 528 + (i & 31) * 16) = tv[it];
  }
  bar();
  const int mis = (k1 & 1) * 2;
  const int l = tid & 63, w = tid >> 6, k2 = l & 15, j1 = 2 * w + ((l >> 4) & 1), h = l >> 5;
  float re[16], im[16];
  unsigned mx = 0;
#pragma unroll
  for (int r = 0; r < 16; ++r) {
    const int hh = r >> 3, kd = j1 + 16 * ((r & 3) + 8 * (r >> 2) + 4 * h);
    const char* p = smem + (hh * 32 + k2) * 528 + mis + (hh ? 512 - kd : kd) * 2;
    re[r] = bf2f(*(const u16*)p);
    const float v = bf2f(*(const u16*)(p + 16 * 528));
    im[r] = hh ? -v : v;
  }
  if (tid < 16) {
#pragma unroll
    for (int hh = 0; hh < 2; ++hh) {
      const char* ob = smem + ((hh ^ 1) * 32 + k2) * 528 + mis + hh * 512;
      const float v = bf2f(*(const u16*)(ob + 16 * 528));
      re[8 * hh] = 0.5f * (re[8 * hh] + bf2f(*(const u16*)ob));
      im[8 * hh] = 0.5f * (im[8 * hh] + (hh ? v : -v));
    }
  }
#pragma unroll
  for (int r = 0; r < 16; ++r) mx = max(mx, max(asU(re[r]) & 0x7fffffffu, asU(im[r]) & 0x7fffffffu));
  mx = wavemax_bits(mx);
  unsigned* red = (unsigned*)(smem + 64 * 528);
  if (l == 0) red[w] = mx;
  bar();
#pragma unroll
  for (int i = 0; i < 8; ++i) mx = max(mx, red[i]);
  const unsigned e = scale_exp(mx);
  const float inv = scale_inv(e);
#pragma unroll
  for (int q = 0; q < 4; ++q)
    wv[q] = u4{pkh(re[4 * q] * inv, re[4 * q + 1] * inv), pkh(re[4 * q + 2] * inv, re[4 * q + 3] * inv),
               pkh(im[4 * q] * inv, im[4 * q + 1] * inv), pkh(im[4 * q + 2] * inv, im[4 * q + 3] * inv)};
  return __builtin_amdgcn_readfirstlane(asU(scale_up(e)));
}

DI void phase1(const u16* __restrict__ wr, const u16* __restrict__ wi, char* ws, char* smem, unsigned tag, int k1, int w, int l) {
  const int i16 = l & 15, g = l >> 4, tid = w * 64 + l;
  const int slot = k1 & 63;
  const bool realonly = (slot == 0);
  char* T1 = smem;
  char* T2 = smem + T1_BYTES;
  char* tab = smem + P1_TAB + l * 16;
  const int n32 = l & 31, h = l >> 5, ro = n32 >> 4, d1 = n32 & 15;
  {
    unsigned c01, c23, s01, s23;
    if (w < 4) {
      const float st = (float)n32 * (1.f / 32.f);
      cs4(st * (8 * w + 4 * h), st, 0.17677669529663687f, c01, c23, s01, s23);
      char* t = tab + w * 4096;
      *(u4*)t = u4{c01, c23, s01, s23};
      *(u4*)(t + 1024) = u4{s01 ^ NEG2, s23 ^ NEG2, c01, c23};
      *(u4*)(t + 2048) = u4{c01, c23, s01 ^ NEG2, s23 ^ NEG2};
      *(u4*)(t + 3072) = u4{s01, s23, c01, c23};
    } else if (w < 6) {
      const float st = (float)i16 * (1.f / 16.f);
      cs4(st * (8 * (g & 1) + 4 * (w & 1)), st, 0.25f, c01, c23, s01, s23);
      char* t = tab + 16 * 1024 + (w & 1) * 8;
      *(u2*)t = (g < 2) ? u2{c01, c23} : u2{s01, s23};
      *(u2*)(t + 1024) = (g < 2) ? u2{s01 ^ NEG2, s23 ^ NEG2} : u2{c01, c23};
      const float s3 = (float)d1 * (1.f / 16.f);
      cs4(s3 * (4 * (2 * (w & 1) + h)), s3, 0.25f, c01, c23, s01, s23);
      *(u4*)(tab + (22 + (w & 1)) * 1024) = ro ? u4{s01, c01, s23, c23} : u4{c01, s01 ^ NEG2, c23, s23 ^ NEG2};
    } else if (w == 6) {
      const float st = (float)(k1 + 128 * i16) * (1.f / 2048.f);
      cs4(st * (4 * g), st, 0.25f, c01, c23, s01, s23);
      *(u4*)(tab + 18 * 1024) = u4{c01, c23, s01, s23};
      *(u4*)(tab + 19 * 1024) = u4{s01 ^ NEG2, s23 ^ NEG2, c01, c23};
    } else {
#pragma unroll
      for (int i = 0; i < 2; ++i) {
        cs4((float)(i16 * (k1 + 1024 * i + 512 * h)) * (1.f / 2048.f), (float)i16 * (1.f / 16.f), 0.25f, c01, c23, s01, s23);
        *(u2*)(tab + 20 * 1024 + i * 8) = ro ? u2{s01, s23} : u2{c01, c23};
        *(u2*)(tab + 21 * 1024 + i * 8) = ro ? u2{c01, c23} : u2{s01 ^ NEG2, s23 ^ NEG2};
      }
    }
  }
  float tc1[4], ts1[4], tc2[2], ts2[2];
#pragma unroll
  for (int t = 0; t < 4; ++t) {
    const float r = (float)((4 * w + t) * i16) * (1.f / 512.f);
    tc1[t] = cosr(r);
    ts1[t] = sinr(r);
  }
#pragma unroll
  for (int t = 0; t < 2; ++t) {
    const float r = (float)(n32 * (2 * w + t)) * (1.f / 512.f);
    tc2[t] = cosr(r);
    ts2[t] = -sinr(r);
  }
  asm volatile("" : "+v"(tc1[0]), "+v"(tc1[1]), "+v"(tc1[2]), "+v"(tc1[3]), "+v"(ts1[0]), "+v"(ts1[1]), "+v"(ts1[2]), "+v"(ts1[3]),
               "+v"(tc2[0]), "+v"(tc2[1]), "+v"(ts2[0]), "+v"(ts2[1]));
  u4 wv[4];
  const unsigned bk = filter_prep(wr, wi, smem, k1, tid, wv);
  if (tid == 0) __hip_atomic_store((gu64*)(ws + OFF_BETA + k1 * 8), ((u64)tag << 32) | bk, RLX);
  STAMP(1);
  if (w == 7) {
    poll<FS>(ws, OFF_FM, 32, tag, l);
    const unsigned mm = ld4(ws, OFF_MMAX + n32 * 4);
    f2 mraw[8];
#pragma unroll
    for (int i = 0; i < 8; ++i)
      mraw[i] = f2{asF(ld4(ws, OFF_MOD + ((2 * i + h) * 64 + n32) * 4)), asF(ld4(ws, OFF_MOD + ((2 * i + h) * 64 + 32 + n32) * 4))};
    const unsigned e = scale_exp(wavemax_bits(mm));
#pragma unroll
    for (int i = 0; i < 8; ++i) {
      const float sc = scale_inv(e) * (i < 4 ? 11.313708498984761f : 1.f);
      *(unsigned*)(smem + P1_MOD + (i >> 2) * 1024 + n32 * 32 + ((2 * i + h) & 7) * 4) = pkh(mraw[i][0] * sc, mraw[i][1] * sc);
    }
    if (l == 0) *(unsigned*)(smem + P1_MOD + 2048) = e;
  } else if (w == 0) {
    poll<FS>(ws, OFF_FA, 128, tag, l);
  }
  bar();
  STAMP(3);
  u4 yv[4];
#pragma unroll
  for (int i = 0; i < 4; ++i) yv[i] = ld16(ws, OFF_Y + slot * 32768 + (4 * i + g) * 2048 + w * 256 + i16 * 16);
  u64 bg0 = __hip_atomic_load((gu64*)(ws + OFF_BETA + l * 8), RLX);
  u64 bg1 = __hip_atomic_load((gu64*)(ws + OFF_BETA + 512), RLX);
  __builtin_amdgcn_sched_barrier(0);
  const unsigned ae = *(const unsigned*)(smem + P1_MOD + 2048);
  h8 mv[4], f32t[16];
  const char* mp = smem + P1_MOD + w * 128 + (g & 1) * 16;
#pragma unroll
  for (int t = 0; t < 4; ++t) mv[t] = *(const h8*)(mp + 1024 + t * 32);
#pragma unroll
  for (int i = 0; i < 16; ++i) f32t[i] = *(const h8*)(tab + i * 1024);
  const h8 f16re = *(const h8*)(tab + 16 * 1024), f16im = *(const h8*)(tab + 17 * 1024);
  const h8 gre = *(const h8*)(tab + 18 * 1024), gim = *(const h8*)(tab + 19 * 1024);
  const h8 ghre = *(const h8*)(tab + 20 * 1024), ghim = *(const h8*)(tab + 21 * 1024);
  h8 if16[2] = {*(const h8*)(tab + 22 * 1024), *(const h8*)(tab + 23 * 1024)};
  __builtin_amdgcn_sched_barrier(0);

  const f4 z = {0.f, 0.f, 0.f, 0.f};
  {
    char* yb = T2 + w * 4096;
#pragma unroll
    for (int i = 0; i < 4; ++i) *(u4*)(yb + (4 * i + g) * 256 + ((i16 ^ (4 * i + g)) * 16)) = yv[i];
    const int go = realonly ? ((g & 1) + (k1 >> 6) * 2) : g;
    h8 a[4];
#pragma unroll
    for (int t = 0; t < 4; ++t) a[t] = *(const h8*)(yb + i16 * 256 + (((4 * t + go) ^ i16) * 16)) * mv[t];
    if (realonly) {
#pragma unroll
      for (int t = 0; t < 4; ++t) {
        if (k1 == 0) a[t] += *(const h8*)(mp + t * 32);
        if (g >= 2) a[t] = asH(u4{0u, 0u, 0u, 0u});
      }
    }
    __builtin_amdgcn_sched_barrier(0);
    STAMP(4);
    f4 dr[4], di[4];
#pragma unroll
    for (int t = 0; t < 4; ++t) { dr[t] = mm16(a[t], f16re, z); di[t] = mm16(a[t], f16im, z); }
    __builtin_amdgcn_sched_barrier(0);
#pragma unroll
    for (int t = 0; t < 4; ++t) {
      const h8 xb = pack8(dr[t], di[t]);
      dr[t] = mm16(gre, xb, z);
      di[t] = mm16(gim, xb, z);
    }
    __builtin_amdgcn_sched_barrier(0);
    f2 pr[4][2], pi[4][2];
#pragma unroll
    for (int t = 0; t < 4; ++t) {
      pr[t][0] = lo2(dr[t]); pr[t][1] = hi2(dr[t]); pi[t][0] = lo2(di[t]); pi[t][1] = hi2(di[t]);
      cmulc(pr[t][0], pi[t][0], tc1[t], ts1[t]);
      cmulc(pr[t][1], pi[t][1], tc1[t], ts1[t]);
    }
#pragma unroll
    for (int e = 0; e < 4; ++e) {
      const int k2 = 4 * g + e;
      const u4 o = {pkh(pr[0][e >> 1][e & 1], pr[1][e >> 1][e & 1]), pkh(pr[2][e >> 1][e & 1], pr[3][e >> 1][e & 1]),
                    pkh(pi[0][e >> 1][e & 1], pi[1][e >> 1][e & 1]), pkh(pi[2][e >> 1][e & 1], pi[3][e >> 1][e & 1])};
      *(u4*)(T1 + i16 * 2064 + k2 * 128 + ((w ^ ((k2 >> 1) & 7)) * 16)) = o;
    }
  }
  bar();
  STAMP(5);

  {
    const char* tb = T1 + (2 * w + ro) * 2064 + i16 * 128;
    h8 bq[4];
#pragma unroll
    for (int kk = 0; kk < 4; ++kk) bq[kk] = *(const h8*)(tb + (((2 * kk + h) ^ ((i16 >> 1) & 7)) * 16));
    f16v xr = zero16(), xi = zero16();
#pragma unroll
    for (int kk = 0; kk < 4; ++kk) {
      xr = mm32h(f32t[4 * kk], bq[kk], xr);
      xi = mm32h(f32t[4 * kk + 1], bq[kk], xi);
    }
    __builtin_amdgcn_sched_barrier(0);
    f16v ur = zero16(), ui = zero16();
#pragma unroll
    for (int q = 0; q < 4; ++q) {
      const h8 wq = asH(wv[q]);
      const h4 r4 = __builtin_bit_cast(h4, u2{pkh(xr[4 * q], xr[4 * q + 1]), pkh(xr[4 * q + 2], xr[4 * q + 3])});
      const h4 i4 = __builtin_bit_cast(h4, u2{pkh(xi[4 * q], xi[4 * q + 1]), pkh(xi[4 * q + 2], xi[4 * q + 3])});
      const h4 wr4 = __builtin_shufflevector(wq, wq, 0, 1, 2, 3), wi4 = __builtin_shufflevector(wq, wq, 4, 5, 6, 7);
      const h4 zr = __builtin_elementwise_fma(r4, wr4, -(i4 * wi4)), zi = __builtin_elementwise_fma(r4, wi4, i4 * wr4);
      const h8 za = __builtin_shufflevector(zr, zi, 0, 1, 2, 3, 4, 5, 6, 7);
      ur = mm32h(za, f32t[4 * q + 2], ur);
      ui = mm32h(za, f32t[4 * q + 3], ui);
      __builtin_amdgcn_sched_barrier(0);
    }
    f16v v[2];
#pragma unroll
    for (int t = 0; t < 2; ++t) {
      const h8 br = frag4(pkh(ur[8 * t], ur[8 * t + 1]), pkh(ur[8 * t + 2], ur[8 * t + 3]), pkh(ur[8 * t + 4], ur[8 * t + 5]), pkh(ur[8 * t + 6], ur[8 * t + 7]));
      const h8 bi = frag4(pkh(ui[8 * t], ui[8 * t + 1]), pkh(ui[8 * t + 2], ui[8 * t + 3]), pkh(ui[8 * t + 4], ui[8 * t + 5]), pkh(ui[8 * t + 6], ui[8 * t + 7]));
      v[t] = mm32h(ghim, bi, mm32h(ghre, br, zero16()));
      __builtin_amdgcn_sched_barrier(0);
    }
    f2 qr[2][4], qi[2][4];
#pragma unroll
    for (int t = 0; t < 2; ++t)
#pragma unroll
      for (int i = 0; i < 4; ++i) {
        qr[t][i] = f2{v[t][2 * i], v[t][2 * i + 1]};
        qi[t][i] = f2{v[t][8 + 2 * i], v[t][8 + 2 * i + 1]};
        cmulc(qr[t][i], qi[t][i], tc2[t], ts2[t]);
      }
    char* wb = T2 + (4 * h * 32 + n32) * 64 + (((w >> 1) ^ ((n32 >> 2) & 3)) * 16) + (w & 1) * 8;
#pragma unroll
    for (int e = 0; e < 8; ++e)
      *(u2*)(wb + (8 * (e >> 2) + (e & 3)) * 2048) = u2{pkh(qr[0][e >> 1][e & 1], qr[1][e >> 1][e & 1]), pkh(qi[0][e >> 1][e & 1], qi[1][e >> 1][e & 1])};
  }
  STAMP(2);
  {
    for (int spin = 0; spin < SPIN_CAP && !__all((bg0 >> 32) == tag && (bg1 >> 32) == tag); ++spin) {
      bg0 = __hip_atomic_load((gu64*)(ws + OFF_BETA + l * 8), RLX);
      bg1 = __hip_atomic_load((gu64*)(ws + OFF_BETA + 512), RLX);
    }
    const unsigned bmax = wavemax_bits(max((unsigned)bg0, (unsigned)bg1));
    const unsigned de = bmax - bk;
    const _Float16 ratio = (_Float16)asF(de < 0x3f800000u ? 0x3f800000u - de : 0u);
    if16[0] *= ratio;
    if16[1] *= ratio;
    if (k1 == 0 && tid == 0) st4(ws, OFF_YSC, asU(asF(bmax) * scale_up(ae)));
  }
  bar();
  STAMP(6);

  const int krow = realonly ? (k1 >> 6) : (2 * slot + ro);
  f16v acc[2];
#pragma unroll
  for (int t = 0; t < 2; ++t) {
    const char* tb = T2 + ((2 * w + t) * 32 + n32) * 64;
    acc[t] = zero16();
#pragma unroll
    for (int st = 0; st < 2; ++st) acc[t] = mm32h(*(const h8*)(tb + (((2 * st + h) ^ ((n32 >> 2) & 3)) * 16)), if16[st], acc[t]);
  }
#pragma unroll
  for (int t = 0; t < 2; ++t)
#pragma unroll
    for (int a = 0; a < 2; ++a) {
      const auto r0 = __builtin_amdgcn_permlane32_swap(pkh(acc[t][8 * a], acc[t][8 * a + 1]), pkh(acc[t][8 * a + 4], acc[t][8 * a + 5]), false, false);
      const auto r1 = __builtin_amdgcn_permlane32_swap(pkh(acc[t][8 * a + 2], acc[t][8 * a + 3]), pkh(acc[t][8 * a + 6], acc[t][8 * a + 7]), false, false);
      const unsigned off = OFF_V + ((2 * w + t) * 128 + krow) * 1024 + (2 * a + h) * 256 + d1 * 16;
      st16(ws, (realonly && ro) ? 0x7fffff00u : off, u4{r0[0], r1[0], r0[1], r1[1]});
    }
}

constexpr int SLOT = 12288, NSLOT = 13, RING = SLOT * NSLOT, NPRE = 11, LDS_BYTES = 163840;
constexpr int P2_IMG = NPRE * SLOT;

DI void glds_v(const char* p, unsigned lds) {
  asm volatile("s_mov_b32 m0, %1\n\ts_nop 0\n\tglobal_load_lds_dwordx4 %0, off" :: "v"(p), "s"(lds) : "memory");
}
DI void glds_s(unsigned voff, const char* base, unsigned lds) {
  asm volatile("s_mov_b32 m0, %2\n\ts_nop 4\n\tglobal_load_lds_dwordx4 %0, %1" :: "v"(voff), "s"(base), "s"(lds) : "memory");
}
DI void ld1(u4& a, unsigned voff, const char* base) {
  asm volatile("global_load_dwordx4 %0, %1, %2 sc1" : "=v"(a) : "v"(voff), "s"(base) : "memory");
}
struct Loader {
  const char* p1[3];
  const char* w2;
  unsigned lds, voff2;
  int lw;
};
DI void load_w1(const Loader& L, int s) {
#pragma unroll
  for (int j = 0; j < 3; ++j) glds_v(L.p1[j] + s * (32 * FH * 2), L.lds + (s % NSLOT) * SLOT + j * 1024);
}
DI int w2_step(int r) { return r + 2 * ((r >= 20) + (r >= 40) + (r >= 59)); }
DI void load_w2(const Loader& L, int p) {
#pragma unroll
  for (int j = 0; j < 3; ++j) {
    const int hs = 12 * p + 3 * L.lw + j;
    glds_s(L.voff2, L.w2 + (w2_step(hs >> 1) * 16 + (hs & 1) * 8) * 1024, L.lds + ((p + 4) % NSLOT) * SLOT + j * 1024);
  }
}

struct P2Pre { u4 xv[2], bfr[8]; };
DI void phase2_pre(const u16* __restrict__ x, P2Pre& P, int s2, int c8, int w, int l) {
  const int n32 = l & 31, h = l >> 5, st = w & 3, dh = w >> 2;
  const int s1 = 32 * st + n32;
  const int d0 = 32 * (8 * (c8 & 1) + 4 * dh + h) + 8 * (c8 >> 1);
  P.xv[0] = *(const u4*)(x + (size_t)(16 * s1 + s2) * 512 + d0);
  P.xv[1] = *(const u4*)(x + (size_t)(16 * s1 + s2) * 512 + d0 + 64);
  float cv[4], sv[4];
#pragma unroll
  for (int e = 0; e < 4; ++e) {
    const float r = (float)((s1 * (4 * h + e)) & 127) * (1.f / 128.f);
    cv[e] = 0.1767766952966369f * cosr(r);
    sv[e] = -0.1767766952966369f * sinr(r);
  }
  const float rr = (float)(s1 & 15) * 0.0625f, rc = cosr(rr), rsn = sinr(rr);
#pragma unroll
  for (int ks = 0; ks < 8; ++ks) {
#pragma unroll
    for (int e = 0; e < 4; ++e) {
      P.bfr[ks][e] = pkh(cv[e], sv[e]);
      if (ks < 7) {
        const float cn = cv[e] * rc + sv[e] * rsn;
        sv[e] = sv[e] * rc - cv[e] * rsn;
        cv[e] = cn;
      }
    }
  }
  if (h == 0) P.bfr[0][0] = pkh(0.08838834764831845f, (s1 & 1) ? -0.08838834764831845f : 0.08838834764831845f);
  asm volatile("" : "+v"(P.bfr[0]), "+v"(P.bfr[1]), "+v"(P.bfr[2]), "+v"(P.bfr[3]), "+v"(P.bfr[4]), "+v"(P.bfr[5]), "+v"(P.bfr[6]),
               "+v"(P.bfr[7]), "+v"(P.xv[0]), "+v"(P.xv[1]));
}

constexpr int P2_MOD = RING;
DI void phase2(const P2Pre& P, char* ws, char* smem, unsigned tag, int s2, int c8, int w, int l) {
  const int tid = w * 64 + l, n32 = l & 31, h = l >> 5, st = w & 3, dh = w >> 2;
  const int s1 = 32 * st + n32;
  const int d0 = 32 * (8 * (c8 & 1) + 4 * dh + h) + 8 * (c8 >> 1);
  f4 ga[4], cb[4];
#pragma unroll
  for (int i = 0; i < 4; ++i) {
    const char* p = smem + P2_MOD + (4 * dh + h + 2 * (i >> 1)) * 32 + (i & 1) * 16;
    ga[i] = *(const f4*)p;
    cb[i] = *(const f4*)(p + 256);
  }
  STAMP(8);
  await(ws, OFF_FB, 65, tag, w, l);
  STAMP(9);
  char* img = smem + P2_IMG;
  const unsigned vo = OFF_V + (s2 * 128 + (tid >> 3)) * 1024 + c8 * 128 + (tid & 7) * 16;
  const u4 v0 = ld16(ws, vo), v1 = ld16(ws, vo + 65536);
  const float ysc = asF(ld4(ws, OFF_YSC));
  {
    const int o = (tid * 16) ^ (((tid >> 4) & 1) * 64);
    *(u4*)(img + o) = v0;
    *(u4*)(img + 8192 + o) = v1;
  }
#pragma unroll
  for (int i = 0; i < 4; ++i) ga[i] *= ysc;
  bar();
  STAMP(10);
  const int q = (l & 15) >> 2, p = l & 3, gi = (l >> 4) & 1;
  const char* a = img + (8 * h + q) * 128 + (((32 * dh + 16 * gi + 8 * (p & 1) + 4 * (p >> 1)) * 2) ^ ((q >> 1) * 64));
  f16v acc = zero16();
#pragma unroll
  for (int ks = 0; ks < 8; ++ks) acc = mm32h(trfrag(a + ks * 2048, a + ks * 2048 + 512), asH(P.bfr[ks]), acc);
  float sm = 0.f, sq = 0.f;
#pragma unroll
  for (int t = 0; t < 2; ++t) {
    u4 o, os;
#pragma unroll
    for (int e = 0; e < 4; ++e) {
      const f4 gv = ga[2 * t + (e >> 1)], cv = cb[2 * t + (e >> 1)];
      const unsigned xx = P.xv[t][e];
      const unsigned r = pkbf(fmaf(acc[8 * t + 2 * e], gv[(2 * e) & 3], bflo(xx)), fmaf(acc[8 * t + 2 * e + 1], gv[(2 * e + 1) & 3], bfhi(xx)));
      o[e] = r;
      const float a0 = bflo(r), a1 = bfhi(r);
      os[e] = pkbf(a0 * cv[(2 * e) & 3], a1 * cv[(2 * e + 1) & 3]);
      sm += a0 + a1;
      sq = fmaf(a0, a0, fmaf(a1, a1, sq));
    }
    const unsigned fo = ((s2 * 4 + st) * 32 + (d0 >> 4) + 4 * t) * 1024 + (n32 + 32 * ((d0 >> 3) & 1)) * 16;
    st16(ws, OFF_XS + fo, os);
    st16(ws, OFF_X1 + fo, o);
  }
  const auto e0 = __builtin_amdgcn_permlane32_swap(asU(sm), asU(sq), false, false);
  st4(ws, OFF_ST + ((s2 * 16 + 2 * c8 + dh) * 128 + s1) * 8 + h * 4, asU(asF(e0[0]) + asF(e0[1])));
}

constexpr int ACT_AHEAD = 8;
template <int N> DI void wait1(u4& a) { asm volatile("s_waitcnt vmcnt(%1)" : "+v"(a) : "i"(N) : "memory"); }

struct G1Ctx { const char *se[3], *so[3], *ab; unsigned voff; };
template <int NT, int I, int N> DI void g1_unit(f16v (&acc)[NT], u4 (&act)[32], h8 (&fr)[2][NT], const u2 (&pq)[6], const u4& bx, const G1Ctx& cx) {
  constexpr int NX = I + 1;
  if constexpr (N < NT) {
    if constexpr (NX < 32) {
      constexpr int O = ((NX / 2) % NSLOT) * SLOT + (NX & 1) * 6144, W = O / 61440;
      const char* p = ((N & 1) ? cx.so[W] : cx.se[W]) + (O - W * 61440) + N * 32;
      fr[NX & 1][N] = trfrag(p, p + 4 * 384);
    }
    if constexpr (N == 1 && I + ACT_AHEAD < 32) ld1(act[I + ACT_AHEAD], cx.voff, cx.ab + (I + ACT_AHEAD) * 1024);
    if constexpr (I < 32) acc[N] = mm32b(__builtin_bit_cast(b8, fr[I & 1][N]), asB(act[I]), acc[N]);
    else acc[N] = mm32b(asB(u4{pq[N][0], pq[N][1], 0u, 0u}), asB(bx), acc[N]);
    __builtin_amdgcn_sched_barrier(0);
    g1_unit<NT, I, N + 1>(acc, act, fr, pq, bx, cx);
  }
}
template <int NT, int I, class Stats>
DI void g1_step(f16v (&acc)[NT], u4 (&act)[32], h8 (&fr)[2][NT], const u2 (&pq)[6], u4& bx, const G1Ctx& cx, Stats& stats) {
  constexpr int NX = I + 1;
  if constexpr (NX < 32 && (NX & 1) == 0) {
    __builtin_amdgcn_s_barrier();
    asm volatile("" ::: "memory");
  }
  if constexpr (I < 32) wait1<(I + ACT_AHEAD <= 32 ? ACT_AHEAD - 1 : 31 - I)>(act[I]);
  if constexpr (I == 12) { stats(); __builtin_amdgcn_sched_barrier(0); }
  if constexpr (I == 1) STAMP(13);
  if constexpr (I == 16) STAMP(14);
  g1_unit<NT, I, 0>(acc, act, fr, pq, bx, cx);
  if constexpr (NX < 33) g1_step<NT, NX>(acc, act, fr, pq, bx, cx, stats);
}

template <int NT> DI void g1_compute(char* ws, char* smem, const u2 (&pq)[6], unsigned tag, int s2, int chunk, int w, int l) {
  const int h = l >> 5, q = (l & 15) >> 2, p = l & 3;
  G1Ctx cx;
  cx.voff = l * 16;
  cx.ab = ws + OFF_XS + (s2 * 4 + w) * 32768;
  {
    const char* b0 = smem + (8 * h + q) * 384 + (p >> 1) * 192 + (p & 1) * 16 + ((l >> 4) & 1) * 8;
#pragma unroll
    for (int i = 0; i < 3; ++i) {
      unsigned e = (unsigned)(size_t)(LDS3 const char*)(b0 + (q >> 1) * 32 + i * 61440), o = e - (q >> 1) * 64;
      asm volatile("" : "+v"(e), "+v"(o));
      cx.se[i] = (const char*)(LDS3 const char*)(size_t)e;
      cx.so[i] = (const char*)(LDS3 const char*)(size_t)o;
    }
  }
  h8 fr[2][NT];
  if (w == 0) poll<FS>(ws, OFF_FC + s2 * 8 * FS, 8, tag, l);
  bar();
  STAMP(20);
  u2 stv[8];
  u4 act[32];
  asm volatile("s_nop 4" ::: "memory");
  ld1(act[0], cx.voff, cx.ab);
  ld1(act[1], cx.voff, cx.ab + 1024);
  {
    const char* sb = ws + OFF_ST + s2 * 16384 + w * 256;
    const unsigned so = (l & 31) * 8 + h * 8192;
    asm volatile("s_nop 4\n\tglobal_load_dwordx2 %0, %8, %9 sc1\n\tglobal_load_dwordx2 %1, %8, %9 offset:1024 sc1\n\t"
                 "global_load_dwordx2 %2, %8, %9 offset:2048 sc1\n\tglobal_load_dwordx2 %3, %8, %9 offset:3072 sc1\n\t"
                 "global_load_dwordx2 %4, %8, %10 sc1\n\tglobal_load_dwordx2 %5, %8, %10 offset:1024 sc1\n\t"
                 "global_load_dwordx2 %6, %8, %10 offset:2048 sc1\n\tglobal_load_dwordx2 %7, %8, %10 offset:3072 sc1"
                 : "=&v"(stv[0]), "=&v"(stv[1]), "=&v"(stv[2]), "=&v"(stv[3]), "=&v"(stv[4]), "=&v"(stv[5]), "=&v"(stv[6]), "=&v"(stv[7])
                 : "v"(so), "s"(sb), "s"(sb + 4096) : "memory");
  }
#pragma unroll
  for (int i = 2; i < ACT_AHEAD; ++i) ld1(act[i], cx.voff, cx.ab + i * 1024);
#pragma unroll
  for (int n = 0; n < NT; ++n) {
    const char* a = ((n & 1) ? cx.so[0] : cx.se[0]) + n * 32;
    fr[0][n] = trfrag(a, a + 4 * 384);
  }
  f16v acc[NT];
#pragma unroll
  for (int n = 0; n < NT; ++n) acc[n] = zero16();
  float sd = 0.f;
  u4 bx;
  auto stats = [&]() {
    asm volatile("" : "+v"(stv[0]), "+v"(stv[1]), "+v"(stv[2]), "+v"(stv[3]), "+v"(stv[4]), "+v"(stv[5]), "+v"(stv[6]), "+v"(stv[7]));
    f2 sv = {0.f, 0.f};
#pragma unroll
    for (int i = 0; i < 8; ++i) sv += __builtin_bit_cast(f2, stv[i]);
    const float sm = sv[0] + __shfl_xor(sv[0], 32), sq = sv[1] + __shfl_xor(sv[1], 32);
    const float mean = sm * (1.f / 512.f);
    const float var = fmaxf(sq * (1.f / 512.f) - mean * mean, 0.f);
    sd = rbf(__builtin_amdgcn_sqrtf(rbf(rbf(var) + 1e-5f)));
    bx = u4{pkbf(-mean, -mean), pkbf(sd, sd), 0u, 0u};
  };
  __builtin_amdgcn_sched_barrier(0);
  wait1<ACT_AHEAD + 7>(act[0]);
  g1_unit<NT, 0, 0>(acc, act, fr, pq, bx, cx);
  g1_step<NT, 1>(acc, act, fr, pq, bx, cx, stats);
  STAMP(15);
  const float rs = __builtin_amdgcn_rcpf(sd), rs2 = rs * rs;
  const unsigned hb = OFF_H + ((s2 * 4 + w) * 86 + chunk * 6) * 1024 + l * 16;
#pragma unroll
  for (int n = 0; n < NT; ++n) {
    u4 o;
#pragma unroll
    for (int e = 0; e < 4; ++e) {
      const int i0 = 8 * (e >> 1) + 2 * (e & 1);
      o[e] = pkbf((acc[n][i0] * rs2) * acc[n][i0 + 4], (acc[n][i0 + 1] * rs2) * acc[n][i0 + 5]);
    }
    st16(ws, hb + n * 1024, o);
  }
  waitv<0>();
}

template <int S> DI void g1_load_step(const Loader& L) {
  if constexpr (S >= NPRE) waitv<30>();
  __builtin_amdgcn_s_barrier();
  asm volatile("" ::: "memory");
  if constexpr (S >= 2 && S <= 4) load_w1(L, S + 11);
  if constexpr (S == 5) load_w2(L, 12);
  if constexpr (S >= 6) load_w2(L, S - 6);
  if constexpr (S < 15) g1_load_step<S + 1>(L);
}

DI void w2_frags(const char* p, int sw, u4& w0, u4& w1) {
  w0 = __builtin_bit_cast(u4, trfrag(p + sw, p + sw + 512));
  w1 = __builtin_bit_cast(u4, trfrag(p + (64 - sw), p + (64 - sw) + 512));
}
DI int w2_lane(int l) { return (8 * (l >> 5) + ((l & 15) >> 2)) * 128 + ((l >> 4) & 1) * 32 + (l & 1) * 16 + ((l >> 1) & 1) * 8; }
template <int I> DI void g2_step(f16v (&acc)[2], u4 (&hf)[22], const char* base, int sw, int r0, bool shortq) {
  int o = (r0 + I) * 2048 + 4 * SLOT;
  o = o >= RING ? o - RING : o;
  u4 w0, w1;
  w2_frags(base + o, sw, w0, w1);
  wait1<21 - I>(hf[I]);
  if (I < 19 || !shortq) {
    acc[0] = mm32b(asB(w0), asB(hf[I]), acc[0]);
    acc[1] = mm32b(asB(w1), asB(hf[I]), acc[1]);
  }
  __builtin_amdgcn_sched_barrier(0);
  if constexpr (I < 19) g2_step<I + 1>(acc, hf, base, sw, r0, shortq);
}

DI void gemm2(char* ws, char* smem, u16* __restrict__ out, const u4 (&wd)[4], unsigned tag, int rt, int ct, int w, int l) {
  const int a = w & 1, kq = ((w >> 1) + ct) & 3, n32 = l & 31, h = l >> 5;
  const unsigned voff = l * 16;
  const bool shortq = kq >= 2;
  const int ks0 = kq < 2 ? 22 * kq : 21 * kq + 2, r0 = ks0 - 2 * kq;
  const int row = (w * 64 + l) >> 3, oct = l & 7, dcol = 64 * ct + 8 * oct;
  u4 xr = ld16(ws, OFF_X1 + ((2 * rt + (row >> 5)) * 32 + (dcol >> 4)) * 1024 + ((row & 31) + 32 * (oct & 1)) * 16);
  f4 g0 = __builtin_bit_cast(f4, ld16(ws, OFF_MOD + (2560 + dcol) * 4)), g1 = __builtin_bit_cast(f4, ld16(ws, OFF_MOD + (2564 + dcol) * 4));
  asm volatile("s_waitcnt vmcnt(0)" : "+v"(xr), "+v"(g0), "+v"(g1) :: "memory");
  STAMP(17);
  await(ws, OFF_FD + (rt >> 1) * 16 * FS, 15, tag, w, l);
  STAMP(18);
  u4 hf[22];
  {
    const char* hb = ws + OFF_H + ((2 * rt + a) * 86 + ks0) * 1024;
    const char* hd = hb - (shortq ? 1024 : 0);
    asm volatile("s_nop 4" ::: "memory");
#pragma unroll
    for (int i = 0; i < 20; ++i) ld1(hf[i], voff, hb + i * 1024);
    ld1(hf[20], voff, hd + 20 * 1024);
    ld1(hf[21], voff, hd + 21 * 1024);
  }
  f16v acc[2] = {zero16(), zero16()};
  g2_step<0>(acc, hf, smem + w2_lane(l), (l & 8) * 8, r0, shortq);
  wait1<1>(hf[20]);
  acc[0] = mm32b(asB(wd[0]), asB(hf[20]), acc[0]);
  acc[1] = mm32b(asB(wd[1]), asB(hf[20]), acc[1]);
  wait1<0>(hf[21]);
  acc[0] = mm32b(asB(wd[2]), asB(hf[21]), acc[0]);
  acc[1] = mm32b(asB(wd[3]), asB(hf[21]), acc[1]);
  STAMP(19);
  bar();
  float* red = (float*)smem;
#pragma unroll
  for (int c = 0; c < 2; ++c)
#pragma unroll
    for (int qd = 0; qd < 4; ++qd)
      *(f4*)(red + (kq * 64 + 32 * a + n32) * 68 + 32 * c + 16 * (qd >> 1) + 8 * h + 4 * (qd & 1)) =
          f4{acc[c][4 * qd], acc[c][4 * qd + 1], acc[c][4 * qd + 2], acc[c][4 * qd + 3]};
  bar();
  f4 s0 = {0.f, 0.f, 0.f, 0.f}, s1 = {0.f, 0.f, 0.f, 0.f};
#pragma unroll
  for (int i = 0; i < 4; ++i) {
    const float* p = red + (i * 64 + row) * 68 + 8 * oct;
    s0 += *(const f4*)p;
    s1 += *(const f4*)(p + 4);
  }
  const u4 o = {pkbf(fmaf(s0[0], g0[0], bflo(xr[0])), fmaf(s0[1], g0[1], bfhi(xr[0]))),
                pkbf(fmaf(s0[2], g0[2], bflo(xr[1])), fmaf(s0[3], g0[3], bfhi(xr[1]))),
                pkbf(fmaf(s1[0], g1[0], bflo(xr[2])), fmaf(s1[1], g1[1], bfhi(xr[2]))),
                pkbf(fmaf(s1[2], g1[2], bflo(xr[3])), fmaf(s1[3], g1[3], bfhi(xr[3])))};
  const int rp = 64 * rt + row;
  *(u4*)(out + (size_t)(16 * (rp & 127) + (rp >> 7)) * 512 + dcol) = o;
}

DI int gemv64_col(int j) { const int sec = j >> 3; return (sec < 2 ? 1536 + 512 * sec : (sec == 2 ? 1024 : 2560)) + 64 * (j & 7); }

__global__ __launch_bounds__(512, 2) void k_fused(const u16* __restrict__ x, const u16* __restrict__ wr, const u16* __restrict__ wi,
                                                  const u16* __restrict__ wg, const u16* __restrict__ wu, const u16* __restrict__ wd,
                                                  const u16* __restrict__ cvec, const u16* __restrict__ wmod, char* ws,
                                                  u16* __restrict__ out) {
  extern __shared__ __attribute__((aligned(16))) char smem[];
  const int b = blockIdx.x, tid = threadIdx.x;
  const int w = __builtin_amdgcn_readfirstlane(tid >> 6), l = tid & 63;
  const int s2 = (b & 7) * 2 + ((b >> 3) & 1), hi = b >> 4;
  if (false) { const u64 t0_ = __builtin_amdgcn_s_memrealtime(); while (__builtin_amdgcn_s_memrealtime() - t0_ < 300) __builtin_amdgcn_s_sleep(2); }
  const unsigned tag = *(__attribute__((address_space(4))) const unsigned*)(uintptr_t)(ws + OFF_EPOCH) + 1;
  if (b < 128) phase0(x, ws, smem, s2, hi, w, l);
  else if (b >= 225) {
#define GV_NAP 0
    for (int i = 0; i < GV_NAP; ++i) __builtin_amdgcn_s_sleep(4);
    mod_gemv<64>(cvec, wmod, ws, smem, gemv64_col(b - 225), tid);
  }
  else if (b >= 193) mod_gemv<32>(cvec, wmod, ws, smem, 32 * (b - 193), tid);
  P2Pre P2;
  if (tid == 0) __hip_atomic_store((gu32*)(ws + OFF_ALIVE + b * 4), tag, RLX);
  if (b < 128) {
    STAMP(3);
    publish(ws, OFF_FA + b * FS, tag);
    STAMP(4);
    phase2_pre(x, P2, s2, hi, w, l);
#define PRE_NAP 0
    for (int i = 0; i < PRE_NAP; ++i) __builtin_amdgcn_s_sleep(4);
  } else if (b < 193) {
    phase1(wr, wi, ws, smem, tag, b - 128, w, l);
    publish(ws, OFF_FB + (b - 128) * FS, tag);
  } else {
    publish(ws, OFF_FM + (b - 193) * FS, tag);
    if (b == 236) {
      mod_gemv<64>(cvec, wmod, ws, smem, gemv64_col(31), tid);
      publish(ws, OFF_FM + 63 * FS, tag);
    } else if (b < 236) {
      pq_job(wg, wu, ws, smem, tag, b - 193, w, l);
      publish(ws, OFF_FQ + (b - 193) * FS, tag);
    }
  }
  STAMP(7);

  const int rt = 2 * s2 + (hi & 1), ct = hi >> 1;
  const int chunk = 4 * (b & 3) + ((b >> 3) & 3), panel = 8 * ((b >> 2) & 1) + (b >> 5);
  Loader L;
  {
    L.lw = w & 3;
    L.lds = (unsigned)(size_t)(LDS3 char*)smem + L.lw * 3072;
#pragma unroll
    for (int j = 0; j < 3; ++j) {
      const int i = 64 * j + l, row = i / 24, r = i - 24 * row, mat = r >= 12;
      const int piece = (r - 12 * mat) ^ (row & 2);
      L.p1[j] = (const char*)((mat ? wu : wg) + (size_t)(8 * L.lw + row) * FH + min(96 * chunk + 8 * piece, FH - 8));
    }
    L.voff2 = (l >> 3) * 1024 + (((l & 7) ^ ((l >> 2) & 4)) * 16);
    L.w2 = (const char*)wd + ct * 128;
  }
  u4 wdir[4];
  {
    const int ks = w2_step(19 + 20 * (w >> 1) - (w >> 2) * ((w >> 1) - 1)) + 1 + (w & 1);
    const unsigned dst = (unsigned)(size_t)(LDS3 char*)smem + P2_IMG + w * 2048;
    glds_s(L.voff2, L.w2 + ks * 16384, dst);
    glds_s(L.voff2, L.w2 + ks * 16384 + 8192, dst + 1024);
    if (w >= 4 && chunk < 15) {
#pragma unroll
      for (int s = 0; s < NPRE; ++s) load_w1(L, s);
      waitv<3 * NPRE>();
    } else {
      waitv<0>();
    }
    if (w == 1) {
      poll<FS, 4>(ws, OFF_FM + 40 * FS, 24, tag, l);
      const int dm = 32 * (8 * (hi & 1) + (l >> 3)) + 8 * ((hi >> 1) & 3) + (l & 7);
      *(unsigned*)(smem + P2_MOD + l * 4) = ld4(ws, OFF_MOD + (1024 + dm) * 4);
      *(unsigned*)(smem + P2_MOD + 256 + l * 4) = ld4(ws, OFF_MOD + (2048 + dm) * 4);
    } else if (w == 2) {
      poll<FS, 4>(ws, OFF_FQ, 43, tag, l);
    }
    bar();
    const char* p = smem + P2_IMG + (((w >> 1) + ct) & 3) * 4096 + w2_lane(l);
    w2_frags(p, (l & 8) * 8, wdir[0], wdir[1]);
    w2_frags(p + 2048, (l & 8) * 8, wdir[2], wdir[3]);
    bar();
  }
  u2 pq[6];
  if (w < 4) {
#pragma unroll
    for (int n = 0; n < 6; ++n) pq[n] = ld8(ws, (l >> 5) ? 0x7fffff00u : OFF_PQ + ((chunk * 6 + n) * 32 + l) * 8);
    asm volatile("" : "+v"(pq[0]), "+v"(pq[1]), "+v"(pq[2]), "+v"(pq[3]), "+v"(pq[4]), "+v"(pq[5]));
  }
  STAMP(11);
  if (b < 128) {
    phase2(P2, ws, smem, tag, s2, hi, w, l);
    publish(ws, OFF_FC + (s2 * 8 + hi) * FS, tag);
  }
  STAMP(12);
  if (chunk < 15) {
    if (w < 4) {
      if (chunk < 14) g1_compute<6>(ws, smem, pq, tag, panel, chunk, w, l);
      else g1_compute<2>(ws, smem, pq, tag, panel, chunk, w, l);
    } else {
      waitv<0>();
      load_w1(L, 11);
      load_w1(L, 12);
      bar();
      if (w == 4) poll<FS>(ws, OFF_FC + s2 * 8 * FS, 8, tag, l);
      g1_load_step<1>(L);
    }
    bar();
    if (tid == 0) __hip_atomic_store((gu32*)(ws + OFF_FD + (panel * 16 + chunk) * FS), tag, RLX);
    STAMP(16);
    if (w >= 4) {
      load_w2(L, 10);
      load_w2(L, 11);
    }
  } else {
    if (w >= 4) {
#pragma unroll
      for (int p = 0; p < NSLOT; ++p) load_w2(L, p);
    }
    if (b == 255 && w == 0) {
      poll<4, 8>(ws, OFF_ALIVE, 128, tag, l);
      poll<4, 8>(ws, OFF_ALIVE + 512, 128, tag, l);
      if (l == 0) __hip_atomic_store((gu32*)(ws + OFF_EPOCH), tag, RLX);
    }
    await(ws, OFF_FC + s2 * 8 * FS, 8, tag, w, l);
  }
  gemm2(ws, smem, out, wdir, tag, rt, ct, w, l);
}

extern "C" void kernel_launch(void* const* d_in, const int* in_sizes, int n_in, void* d_out, int out_size,
                              void* d_ws, size_t ws_size, hipStream_t stream) {
  (void)hipFuncSetAttribute((const void*)k_fused, hipFuncAttributeMaxDynamicSharedMemorySize, LDS_BYTES);
  k_fused<<<256, 512, LDS_BYTES, stream>>>((const u16*)d_in[0], (const u16*)d_in[1], (const u16*)d_in[2], (const u16*)d_in[4],
                                           (const u16*)d_in[5], (const u16*)d_in[6], (const u16*)d_in[7], (const u16*)d_in[8],
                                           (char*)d_ws, (u16*)d_out);
}
```

```cpp
#include <hip/hip_runtime.h>
#include <hip/hip_bf16.h>
#include <stdint.h>

typedef unsigned short u16;
typedef unsigned long long u64;
typedef _Float16 h2 __attribute__((ext_vector_type(2)));
typedef _Float16 h4 __attribute__((ext_vector_type(4)));
typedef _Float16 h8 __attribute__((ext_vector_type(8)));
typedef __bf16 b2 __attribute__((ext_vector_type(2)));
typedef __bf16 b8 __attribute__((ext_vector_type(8)));
typedef float f2 __attribute__((ext_vector_type(2)));
typedef float f4 __attribute__((ext_vector_type(4)));
typedef float f16v __attribute__((ext_vector_type(16)));
typedef unsigned u2 __attribute__((ext_vector_type(2)));
typedef unsigned u4 __attribute__((ext_vector_type(4)));
typedef unsigned u4a __attribute__((ext_vector_type(4), aligned(4)));
typedef short s4 __attribute__((ext_vector_type(4)));
typedef __attribute__((address_space(1))) unsigned gu32;
typedef __attribute__((address_space(1))) u64 gu64;
#define DI __device__ __forceinline__
#define LDS3 __attribute__((address_space(3)))
#define RLX __ATOMIC_RELAXED, __HIP_MEMORY_SCOPE_AGENT

constexpr int FH = 1376;
constexpr unsigned OFF_MOD = 0;
constexpr unsigned OFF_YSC = 14336;
constexpr unsigned OFF_EPOCH = 20480;
constexpr unsigned OFF_ALIVE = OFF_EPOCH + 1024;
constexpr unsigned OFF_BETA = OFF_ALIVE + 1024;
#define FSTR 64
constexpr unsigned FS = FSTR;
constexpr unsigned OFF_FA = OFF_BETA + 1024;
constexpr unsigned OFF_FM = OFF_FA + 128 * FS;
constexpr unsigned OFF_FQ = OFF_FM + 64 * FS, OFF_FB = OFF_FQ + 48 * FS, OFF_FC = OFF_FB + 72 * FS;
constexpr unsigned OFF_FX = OFF_FC + 128 * FS, OFF_FD = OFF_FX + 128 * FS;
constexpr unsigned OFF_MG = 212992;
constexpr unsigned OFF_PQ = 229376;
constexpr unsigned OFF_Y = 262144;
constexpr unsigned OFF_V = OFF_Y + 2097152;
constexpr unsigned OFF_X1 = OFF_V + 2097152;
constexpr unsigned OFF_XS = OFF_X1 + 2097152;
constexpr unsigned OFF_ST = OFF_XS + 2097152;
constexpr unsigned OFF_H = OFF_ST + 262144;
static_assert(OFF_FD + 256 * FS <= OFF_MG && OFF_PQ + 88 * 256 <= OFF_Y, "workspace overlap");

#define STAMP(idx)
#define STAMPC(idx)

DI float bf2f(unsigned v) { return __builtin_bit_cast(float, v << 16); }
DI float bflo(unsigned v) { return __builtin_bit_cast(float, v << 16); }
DI float bfhi(unsigned v) { return __builtin_bit_cast(float, v & 0xffff0000u); }
DI unsigned pkbf(float a, float b) { f2 v = {a, b}; return __builtin_bit_cast(unsigned, __builtin_convertvector(v, b2)); }
DI unsigned pkh(float a, float b) { f2 v = {a, b}; return __builtin_bit_cast(unsigned, __builtin_convertvector(v, h2)); }
DI float rbf(float a) { return bflo(pkbf(a, a)); }
DI float cosr(float rev) { return __builtin_amdgcn_cosf(rev); }
DI float sinr(float rev) { return __builtin_amdgcn_sinf(rev); }
DI b2 asB2(unsigned v) { return __builtin_bit_cast(b2, v); }
DI unsigned asU(float v) { return __builtin_bit_cast(unsigned, v); }
DI float asF(unsigned v) { return __builtin_bit_cast(float, v); }
DI unsigned bfi(unsigned m, unsigned a, unsigned b) { return (m & a) | (~m & b); }
DI h8 asH(u4 v) { return __builtin_bit_cast(h8, v); }
DI b8 asB(u4 v) { return __builtin_bit_cast(b8, v); }
DI f4 mm16(h8 a, h8 b, f4 c) { return __builtin_amdgcn_mfma_f32_16x16x32_f16(a, b, c, 0, 0, 0); }
DI f16v mm32h(h8 a, h8 b, f16v c) { return __builtin_amdgcn_mfma_f32_32x32x16_f16(a, b, c, 0, 0, 0); }
DI f16v mm32b(b8 a, b8 b, f16v c) { return __builtin_amdgcn_mfma_f32_32x32x16_bf16(a, b, c, 0, 0, 0); }
DI f16v zero16() { f16v z; _Pragma("unroll") for (int e = 0; e < 16; ++e) z[e] = 0.f; return z; }
DI h8 pack8(f4 r, f4 i) { u4 v = {pkh(r[0], r[1]), pkh(r[2], r[3]), pkh(i[0], i[1]), pkh(i[2], i[3])}; return asH(v); }
template <int CTRL> DI float dppadd(float v) { return v + __builtin_amdgcn_update_dpp(0.f, v, CTRL, 0xf, 0xf, false); }
template <int CTRL> DI unsigned dppmax(unsigned v) { return max(v, (unsigned)__builtin_amdgcn_update_dpp(0, (int)v, CTRL, 0xf, 0xf, false)); }
DI unsigned wavemax_bits(unsigned v) {
  v = dppmax<0x128>(v); v = dppmax<0x124>(v); v = dppmax<0x122>(v); v = dppmax<0x121>(v);
  return max(max((unsigned)__builtin_amdgcn_readlane(v, 0), (unsigned)__builtin_amdgcn_readlane(v, 16)),
             max((unsigned)__builtin_amdgcn_readlane(v, 32), (unsigned)__builtin_amdgcn_readlane(v, 48)));
}
DI unsigned scale_exp(unsigned bits) { return min(max((bits >> 23) & 0xffu, 1u), 252u); }
DI float scale_up(unsigned e) { return asF((e + 1) << 23); }
DI float scale_inv(unsigned e) { return asF((253 - e) << 23); }
DI f2 lo2(f4 v) { return __builtin_shufflevector(v, v, 0, 1); }
DI f2 hi2(f4 v) { return __builtin_shufflevector(v, v, 2, 3); }
DI h8 frag4(unsigned a, unsigned b, unsigned c, unsigned d) { u4 v = {a, b, c, d}; return asH(v); }
DI s4 trread(const char* p) { return __builtin_amdgcn_ds_read_tr16_b64_v4i16((LDS3 s4*)p); }
DI h8 trfrag(const char* p0, const char* p1) {
  const u2 lo = __builtin_bit_cast(u2, trread(p0)), hi = __builtin_bit_cast(u2, trread(p1));
  return frag4(lo[0], lo[1], hi[0], hi[1]);
}
constexpr unsigned NEG2 = 0x80008000u;
DI void cs4(float r0, float step, float sc, unsigned& c01, unsigned& c23, unsigned& s01, unsigned& s23) {
  float c[4], s[4];
#pragma unroll
  for (int i = 0; i < 4; ++i) {
    const float r = __builtin_amdgcn_fractf(r0 + step * i);
    c[i] = sc * cosr(r);
    s[i] = sc * sinr(r);
  }
  c01 = pkh(c[0], c[1]); c23 = pkh(c[2], c[3]); s01 = pkh(s[0], s[1]); s23 = pkh(s[2], s[3]);
}

DI __amdgpu_buffer_rsrc_t wsr(char* ws) { return __builtin_amdgcn_make_buffer_rsrc(ws, 0, 0x4000000, 0x00020000); }
DI void st16(char* ws, unsigned off, u4 v) { __builtin_amdgcn_raw_buffer_store_b128(v, wsr(ws), off, 0, 16); }
DI void st8(char* ws, unsigned off, u2 v) { __builtin_amdgcn_raw_buffer_store_b64(v, wsr(ws), off, 0, 16); }
DI void st4(char* ws, unsigned off, unsigned v) { __builtin_amdgcn_raw_buffer_store_b32(v, wsr(ws), off, 0, 16); }
DI u4 ld16(char* ws, unsigned off) { return __builtin_amdgcn_raw_buffer_load_b128(wsr(ws), off, 0, 16); }
DI u2 ld8(char* ws, unsigned off) { return __builtin_amdgcn_raw_buffer_load_b64(wsr(ws), off, 0, 16); }
DI unsigned ld4(char* ws, unsigned off) { return __builtin_amdgcn_raw_buffer_load_b32(wsr(ws), off, 0, 16); }
template <int N> DI void waitv() { asm volatile("s_waitcnt vmcnt(%0)" :: "i"(N) : "memory"); }
DI void bar() { asm volatile("s_waitcnt lgkmcnt(0)" ::: "memory"); __builtin_amdgcn_s_barrier(); asm volatile("" ::: "memory"); }
constexpr int SPIN_CAP = 1 << 16;
DI void publish(char* ws, unsigned flag_off, unsigned tag) {
  waitv<0>();
  bar();
  if (threadIdx.x == 0) __hip_atomic_store((gu32*)(ws + flag_off), tag, RLX);
}
template <unsigned STRIDE, int NAP = 1> DI void poll(char* ws, unsigned off, int n, unsigned tag, int l) {
  gu32* p0 = (gu32*)(ws + off + min(l, n - 1) * STRIDE);
  gu32* p1 = (gu32*)(ws + off + min(l + 64, n - 1) * STRIDE);
  for (int spin = 0; spin < SPIN_CAP; ++spin) {
    bool ok = __hip_atomic_load(p0, RLX) == tag;
    if (n > 64) ok &= __hip_atomic_load(p1, RLX) == tag;
    if (__all(ok)) break;
    __builtin_amdgcn_s_sleep(NAP);
  }
  __builtin_amdgcn_fence(__ATOMIC_ACQUIRE, "wavefront");
}
DI void await(char* ws, unsigned off, int n, unsigned tag, int w, int l) {
  if (w == 0) poll<FS>(ws, off, n, tag, l);
  bar();
}

constexpr int P0_RPL = 16 * 136;
constexpr int P0_RAW = 8 * P0_RPL + 4 * (P0_RPL + 32);
constexpr int P0_STAT = P0_RAW + 128 * 128;

DI void p0_dft(u4 (&bfr)[8], int m, int h) {
  const int slot = m >> 1, keff = (m == 1) ? 64 : slot;
  const float st = (float)keff * (1.f / 128.f), ph = ((m & 1) && slot) ? 0.25f : 0.f;
#pragma unroll
  for (int k0 = 0; k0 < 2; ++k0) {
    const float r0 = st * (16 * k0 + 8 * h) + ph;
    u4 cc, ss;
    unsigned a0, a1, a2, a3;
    cs4(r0, st, 0.08838834764831845f, a0, a1, a2, a3);
    cc[0] = a0; cc[1] = a1; ss[0] = a2; ss[1] = a3;
    cs4(r0 + 4.f * st, st, 0.08838834764831845f, a0, a1, a2, a3);
    cc[2] = a0; cc[3] = a1; ss[2] = a2; ss[3] = a3;
    bfr[k0] = cc;
#pragma unroll
    for (int kq = 1; kq < 4; ++kq) {
      const int q = (keff * kq) & 3;
      const unsigned sg = ((q + 1) & 2) ? NEG2 : 0u;
#pragma unroll
      for (int e = 0; e < 4; ++e) bfr[2 * kq + k0][e] = ((q & 1) ? ss[e] : cc[e]) ^ sg;
    }
  }
  asm volatile("" : "+v"(bfr[0]), "+v"(bfr[1]), "+v"(bfr[2]), "+v"(bfr[3]), "+v"(bfr[4]), "+v"(bfr[5]), "+v"(bfr[6]), "+v"(bfr[7]));
}

DI void p0_load(const u16* __restrict__ x, u4 (&xv)[16], int s2, int c, int w, int l) {
  const u16* xr = x + (size_t)(16 * (16 * ((w + c) & 7) + ((l >> 2) & 3)) + s2) * 512 + 32 * (l >> 4) + 8 * (l & 3);
#pragma unroll
  for (int q = 0; q < 4; ++q)
#pragma unroll
    for (int G = 0; G < 4; ++G) xv[4 * G + q] = *(const u4*)(xr + G * (64 * 512) + 128 * ((q + w) & 3));
}
DI void phase0(const u16* __restrict__ x, u4 (&ld)[16], char* ws, char* smem, int s2, int c, int w, int l) {
  const int i16 = l & 15, g = l >> 4, n32 = l & 31, h = l >> 5;
  const int rg = (w + c) & 7;
  const int r4 = (l >> 2) & 3, pc = l & 3;
  u4 bfr[8];
  STAMP(5);
  p0_dft(bfr, 32 * (w & 3) + n32, h);
  __builtin_amdgcn_sched_barrier(0);
  STAMP(6);
  if (w >= 4) p0_load(x, ld, s2, c, w, l);
  STAMP(23);
  u4 (&xv)[4][4] = reinterpret_cast<u4 (&)[4][4]>(ld);
  unsigned mh = (c & 1) ? ~0u : 0u;
  asm volatile("" : "+s"(mh));
  __builtin_amdgcn_sched_barrier(0);

  char* raw = smem + w * P0_RPL;
  char* rawp = ((pc == (c >> 1)) ? raw : smem + 8 * P0_RPL + pc * (P0_RPL + 32)) + r4 * 136 + g * 8;
  float* stat = (float*)(smem + P0_STAT) + w * 32;
  const u4 ones = {0x3f803f80u, 0x3f803f80u, 0x3f803f80u, 0x3f803f80u};
  f4 as[4], aq[4];
#pragma unroll
  for (int G = 0; G < 4; ++G) {
    as[G] = f4{0.f, 0.f, 0.f, 0.f};
    aq[G] = f4{0.f, 0.f, 0.f, 0.f};
  }
#pragma unroll
  for (int q = 0; q < 4; ++q) {
#pragma unroll
    for (int G = 0; G < 4; ++G) {
      as[G] = __builtin_amdgcn_mfma_f32_16x16x32_bf16(asB(xv[G][q]), asB(ones), as[G], 0, 0, 0);
      aq[G] = __builtin_amdgcn_mfma_f32_16x16x32_bf16(asB(xv[G][q]), asB(xv[G][q]), aq[G], 0, 0, 0);
      *(u2*)(rawp + G * (4 * 136) + ((q + w) & 3) * 32) = u2{bfi(mh, xv[G][q][2], xv[G][q][0]), bfi(mh, xv[G][q][3], xv[G][q][1])};
      __builtin_amdgcn_sched_barrier(0);
    }
  }
  const int row = l >> 2;
  u2 rv[4];
#pragma unroll
  for (int i = 0; i < 4; ++i) rv[i] = *(const u2*)(raw + row * 136 + pc * 32 + i * 8);
  {
    float dq[4], ds[4];
#pragma unroll
    for (int G = 0; G < 4; ++G) {
      dq[G] = (pc & 2) ? ((pc & 1) ? aq[G][3] : aq[G][2]) : ((pc & 1) ? aq[G][1] : aq[G][0]);
      ds[G] = (as[G][0] + as[G][1]) + (as[G][2] + as[G][3]);
    }
#pragma unroll
    for (int G = 0; G < 4; ++G) dq[G] = dppadd<0xB1>(dq[G]);
#pragma unroll
    for (int G = 0; G < 4; ++G) dq[G] = dppadd<0x4E>(dq[G]);
    const float q1 = (pc & 2) ? ((pc & 1) ? dq[3] : dq[2]) : ((pc & 1) ? dq[1] : dq[0]);
    const float s1 = (pc & 2) ? ((pc & 1) ? ds[3] : ds[2]) : ((pc & 1) ? ds[1] : ds[0]);
    if (r4 == g) *(f2*)(stat + (4 * pc + g) * 2) = f2{s1, q1};
  }
  STAMP(1);
  const f2 sv = *(const f2*)(stat + row * 2);
  const float mean = sv[0] * (1.f / 512.f);
  const float var = fmaxf(sv[1] * (1.f / 512.f) - mean * mean, 0.f);
  const float sd = rbf(__builtin_amdgcn_sqrtf(rbf(rbf(var) + 1e-5f)));
  const float rs = __builtin_amdgcn_rcpf(sd);
  const float nb = -rbf(mean) * rs;
  char* nimg = smem + P0_RAW;
  {
    const int s1 = 16 * rg + row;
    char* nb_ = nimg + s1 * 128 + pc * 32;
    const int sw = s1 & 2;
#pragma unroll
    for (int tp = 0; tp < 2; ++tp) {
      u4 o;
#pragma unroll
      for (int t = 0; t < 2; ++t) {
        float v[4];
#pragma unroll
        for (int i = 0; i < 4; ++i) v[i] = fmaf(t ? bfhi(rv[i][tp]) : bflo(rv[i][tp]), rs, nb);
        o[2 * t] = pkh(v[0], v[1]);
        o[2 * t + 1] = pkh(v[2], v[3]);
      }
      *(u4*)(nb_ + (((2 * tp) ^ sw) * 8)) = o;
    }
  }
  bar();
  STAMP(2);
  {
    const int q = i16 >> 2, p4 = i16 & 3, tp = w >> 2;
    const char* a = nimg + (8 * h + q) * 128 + p4 * 32 + (((2 * tp + (g & 1)) ^ (q & 2)) * 8);
    f16v acc = zero16();
#pragma unroll
    for (int ks = 0; ks < 8; ++ks) acc = mm32h(trfrag(a + ks * 2048, a + ks * 2048 + 512), asH(bfr[ks]), acc);
    char* yb = raw + (n32 >> 1) * 136 + (n32 & 1) * 32 + h * 8;
#pragma unroll
    for (int qd = 0; qd < 4; ++qd)
      *(u2*)(yb + (qd >> 1) * 64 + (qd & 1) * 16) = u2{pkh(acc[4 * qd], acc[4 * qd + 1]), pkh(acc[4 * qd + 2], acc[4 * qd + 3])};
#pragma unroll
    for (int i = 0; i < 2; ++i) {
      const int sl = 8 * i + (l >> 3);
      const char* p = raw + sl * 136 + (l & 7) * 16;
      const u2 lo = *(const u2*)p, hi = *(const u2*)(p + 8);
      st16(ws, OFF_Y + (16 * (w & 3) + sl) * 32768 + s2 * 2048 + c * 256 + tp * 128 + (l & 7) * 16, u4{lo[0], lo[1], hi[0], hi[1]});
    }
  }
}

template <int NC> DI void gemv_load(const u16* __restrict__ cvec, const u16* __restrict__ wmod, u4 (&ld)[16], int col0, int tid) {
  constexpr int OC = NC / 8, RG = 512 / OC;
#pragma unroll
  for (int i = 0; i < OC; ++i) {
    ld[i] = *(const u4*)(wmod + (size_t)(tid / OC + RG * i) * 3072 + col0 + 8 * (tid % OC));
    ld[8 + i / 4][i & 3] = cvec[tid / OC + RG * i];
  }
}
template <int NC> DI void mod_gemv(const u4 (&wv)[16], char* ws, char* smem, unsigned tag, int col0, int tid) {
  constexpr int OC = NC / 8;
  float acc[8];
#pragma unroll
  for (int e = 0; e < 8; ++e) acc[e] = 0.f;
#pragma unroll
  for (int i = 0; i < OC; ++i) {
    const float cf = bf2f(wv[8 + i / 4][i & 3]);
    const float sl = rbf(cf * __builtin_amdgcn_rcpf(1.f + __expf(-cf)));
#pragma unroll
    for (int e = 0; e < 4; ++e) {
      acc[2 * e] = fmaf(sl, bflo(wv[i][e]), acc[2 * e]);
      acc[2 * e + 1] = fmaf(sl, bfhi(wv[i][e]), acc[2 * e + 1]);
    }
  }
#pragma unroll
  for (int e = 0; e < 8; ++e) {
    acc[e] = dppadd<0x128>(acc[e]);
    if (OC == 4) acc[e] = dppadd<0x124>(acc[e]);
  }
  float* red = (float*)smem;
  if ((tid & 15) < OC) {
    *(f4*)(red + (tid >> 4) * (NC + 4) + 8 * (tid & 15)) = f4{acc[0], acc[1], acc[2], acc[3]};
    *(f4*)(red + (tid >> 4) * (NC + 4) + 8 * (tid & 15) + 4) = f4{acc[4], acc[5], acc[6], acc[7]};
  }
  bar();
  if (tid < 64) {
    const int cidx = tid % NC;
    constexpr int PER = NC / 2;
    float v = 0.f;
#pragma unroll
    for (int i = 0; i < PER; ++i) v += red[(PER * (tid / NC) + i) * (NC + 4) + cidx];
    if (NC == 32) {
      const auto e = __builtin_amdgcn_permlane32_swap(asU(v), asU(v), false, false);
      v = asF(e[0]) + asF(e[1]);
    }
    v = rbf(v);
    const int sec = col0 >> 9;
    if (sec == 1 || sec == 4) v = rbf(1.f + v);
    if (NC == 64) st4(ws, OFF_MOD + (col0 + cidx) * 4, asU(v));
    else if (tid < NC) __hip_atomic_store((gu64*)(ws + OFF_MG + (col0 + cidx) * 8), ((u64)tag << 32) | asU(v), RLX);
  }
}

DI void pq_job(const u16* __restrict__ wg, const u16* __restrict__ wu, char* ws, char* smem, unsigned tag, int j, int w, int l) {
  const int tid = w * 64 + l, oct = tid & 7, rg = tid >> 3;
  const u16* src = ((oct & 4) ? wu : wg) + 32 * j + 8 * (oct & 3);
  u4 wv[8];
#pragma unroll
  for (int i = 0; i < 8; ++i) wv[i] = *(const u4*)(src + (size_t)(rg + 64 * i) * FH);
  await(ws, OFF_FM + 32 * FS, 16, tag, w, l);
  float pa[8], qa[8];
#pragma unroll
  for (int e = 0; e < 8; ++e) { pa[e] = 0.f; qa[e] = 0.f; }
#pragma unroll
  for (int i = 0; i < 8; ++i) {
    const float cb = asF(ld4(ws, OFF_MOD + (2048 + rg + 64 * i) * 4)), sb = asF(ld4(ws, OFF_MOD + (1536 + rg + 64 * i) * 4));
#pragma unroll
    for (int e = 0; e < 4; ++e) {
      const float v0 = bflo(wv[i][e]), v1 = bfhi(wv[i][e]);
      pa[2 * e] = fmaf(cb, v0, pa[2 * e]); pa[2 * e + 1] = fmaf(cb, v1, pa[2 * e + 1]);
      qa[2 * e] = fmaf(sb, v0, qa[2 * e]); qa[2 * e + 1] = fmaf(sb, v1, qa[2 * e + 1]);
    }
  }
  float* red = (float*)smem;
  *(f4*)(red + rg * 132 + 8 * oct) = f4{pa[0], pa[1], pa[2], pa[3]};
  *(f4*)(red + rg * 132 + 8 * oct + 4) = f4{pa[4], pa[5], pa[6], pa[7]};
  *(f4*)(red + rg * 132 + 64 + 8 * oct) = f4{qa[0], qa[1], qa[2], qa[3]};
  *(f4*)(red + rg * 132 + 68 + 8 * oct) = f4{qa[4], qa[5], qa[6], qa[7]};
  bar();
  float v = 0.f;
#pragma unroll
  for (int i = 0; i < 16; ++i) v += red[(16 * (tid >> 7) + i) * 132 + (tid & 127)];
  float* red2 = red + 64 * 132;
  red2[tid] = v;
  bar();
  if (tid < 128) {
    v = (red2[tid] + red2[128 + tid]) + (red2[256 + tid] + red2[384 + tid]);
    const float hi = rbf(v);
    const int f = tid & 15, mat = (tid >> 5) & 1;
    const int r = (f & 3) + 4 * (f >> 3) + 8 * mat + 16 * ((f >> 2) & 1);
    st4(ws, OFF_PQ + ((2 * j + ((tid >> 4) & 1)) * 32 + r) * 8 + (tid >> 6) * 4, pkbf(hi, v - hi));
  }
}

constexpr int T1_BYTES = 16 * 2064, T2_BYTES = 32768;
constexpr int P1_TAB = T1_BYTES + T2_BYTES, P1_MOD = P1_TAB + 24 * 1024;

DI void cmulc(f2& r, f2& i, float tc, float ts) {
  const f2 a = r * tc + i * ts;
  i = i * tc - r * ts;
  r = a;
}

DI const char* filter_row(const u16* wr, const u16* wi, int k1, int row) {
  const int ksA = k1 + 128 * (row & 15);
  const int ks = (row >> 5) ? ((2048 - ksA) & 2047) : ksA;
  return (const char*)(((row >> 4) & 1) ? wi : wr) + ((ks * 514) & ~3);
}
DI void filter_gather(const char* smem, int k1, int tid, float inv, u4 (&wv)[4]) {
  const int mis = (k1 & 1) * 2;
  const int l = tid & 63, w = tid >> 6, k2 = l & 15, j1 = 2 * w + ((l >> 4) & 1), h = l >> 5;
  const char* pa = smem + k2 * 528 + mis + (j1 + 64 * h) * 2;
  const char* pb = smem + (32 + k2) * 528 + mis + (256 - j1 - 64 * h) * 2;
  float re[16], im[16];
#pragma unroll
  for (int r = 0; r < 16; ++r) {
    const int o = 32 * ((r & 3) + 8 * ((r >> 2) & 1));
    const char* p = (r >> 3) ? pb - o : pa + o;
    re[r] = bf2f(*(const u16*)p);
    im[r] = bf2f(*(const u16*)(p + 16 * 528));
  }
  if (tid < 16) {
#pragma unroll
    for (int hh = 0; hh < 2; ++hh) {
      const char* ob = smem + ((hh ^ 1) * 32 + k2) * 528 + mis + hh * 512;
      re[8 * hh] = 0.5f * (re[8 * hh] + bf2f(*(const u16*)ob));
      im[8 * hh] = 0.5f * (im[8 * hh] - bf2f(*(const u16*)(ob + 16 * 528)));
    }
  }
#pragma unroll
  for (int q = 0; q < 4; ++q) {
    const float si = q < 2 ? inv : -inv;
    wv[q] = u4{pkh(re[4 * q] * inv, re[4 * q + 1] * inv), pkh(re[4 * q + 2] * inv, re[4 * q + 3] * inv),
               pkh(im[4 * q] * si, im[4 * q + 1] * si), pkh(im[4 * q + 2] * si, im[4 * q + 3] * si)};
  }
}

DI void p1_load(const u16* __restrict__ wr, const u16* __restrict__ wi, u4 (&ld)[16], int k1, int tid) {
#pragma unroll
  for (int it = 0; it < 4; ++it) ld[it] = *(const u4a*)(filter_row(wr, wi, k1, (tid >> 5) + 16 * it) + (tid & 31) * 16);
  ld[4][0] = *(const unsigned*)(filter_row(wr, wi, k1, tid & 63) + 512);
}
DI void phase1(const u4 (&tv)[16], char* ws, char* smem, unsigned tag, int k1, int w, int l) {
  const int i16 = l & 15, g = l >> 4, tid = w * 64 + l;
  const int slot = k1 & 63;
  const bool realonly = (slot == 0);
  char* T1 = smem;
  char* T2 = smem + T1_BYTES;
  char* tab = smem + P1_TAB + l * 16;
  const int n32 = l & 31, h = l >> 5, ro = n32 >> 4, d1 = n32 & 15;
  {
    unsigned c01, c23, s01, s23;
    if (w < 4) {
      const float st = (float)n32 * (1.f / 32.f);
      cs4(st * (8 * w + 4 * h), st, 0.17677669529663687f, c01, c23, s01, s23);
      char* t = tab + w * 4096;
      *(u4*)t = u4{c01, c23, s01, s23};
      *(u4*)(t + 1024) = u4{s01 ^ NEG2, s23 ^ NEG2, c01, c23};
      *(u4*)(t + 2048) = u4{c01, c23, s01 ^ NEG2, s23 ^ NEG2};
      *(u4*)(t + 3072) = u4{s01, s23, c01, c23};
    } else if (w < 6) {
      const float st = (float)i16 * (1.f / 16.f);
      cs4(st * (8 * (g & 1) + 4 * (w & 1)), st, 0.25f, c01, c23, s01, s23);
      char* t = tab + 16 * 1024 + (w & 1) * 8;
      *(u2*)t = (g < 2) ? u2{c01, c23} : u2{s01, s23};
      *(u2*)(t + 1024) = (g < 2) ? u2{s01 ^ NEG2, s23 ^ NEG2} : u2{c01, c23};
      const float s3 = (float)d1 * (1.f / 16.f);
      cs4(s3 * (4 * (2 * (w & 1) + h)), s3, 0.25f, c01, c23, s01, s23);
      *(u4*)(tab + (22 + (w & 1)) * 1024) = ro ? u4{s01, c01, s23, c23} : u4{c01, s01 ^ NEG2, c23, s23 ^ NEG2};
    } else if (w == 6) {
      const float st = (float)(k1 + 128 * i16) * (1.f / 2048.f);
      cs4(st * (4 * g), st, 0.25f, c01, c23, s01, s23);
      *(u4*)(tab + 18 * 1024) = u4{c01, c23, s01, s23};
      *(u4*)(tab + 19 * 1024) = u4{s01 ^ NEG2, s23 ^ NEG2, c01, c23};
    } else {
#pragma unroll
      for (int i = 0; i < 2; ++i) {
        cs4((float)(i16 * (k1 + 1024 * i + 512 * h)) * (1.f / 2048.f), (float)i16 * (1.f / 16.f), 0.25f, c01, c23, s01, s23);
        *(u2*)(tab + 20 * 1024 + i * 8) = ro ? u2{s01, s23} : u2{c01, c23};
        *(u2*)(tab + 21 * 1024 + i * 8) = ro ? u2{c01, c23} : u2{s01 ^ NEG2, s23 ^ NEG2};
      }
    }
  }
  float tc1[4], ts1[4], tc2[2], ts2[2];
#pragma unroll
  for (int t = 0; t < 4; ++t) {
    const float r = (float)((4 * w + t) * i16) * (1.f / 512.f);
    tc1[t] = cosr(r);
    ts1[t] = sinr(r);
  }
#pragma unroll
  for (int t = 0; t < 2; ++t) {
    const float r = (float)(n32 * (2 * w + t)) * (1.f / 512.f);
    tc2[t] = cosr(r);
    ts2[t] = -sinr(r);
  }
  asm volatile("" : "+v"(tc1[0]), "+v"(tc1[1]), "+v"(tc1[2]), "+v"(tc1[3]), "+v"(ts1[0]), "+v"(ts1[1]), "+v"(ts1[2]), "+v"(ts1[3]),
               "+v"(tc2[0]), "+v"(tc2[1]), "+v"(ts2[0]), "+v"(ts2[1]));
  gu64* mg = (gu64*)(ws + OFF_MG + ((tid >> 5) * 64 + n32) * 8);
  unsigned* red = (unsigned*)(smem + 64 * 528);
  {
    typedef unsigned short us2 __attribute__((ext_vector_type(2)));
    us2 mx = __builtin_bit_cast(us2, tv[4][0] & 0x7fff7fffu);
#pragma unroll
    for (int it = 0; it < 4; ++it) {
      *(u4*)(smem + ((tid >> 5) + 16 * it) * 528 + (tid & 31) * 16) = tv[it];
#pragma unroll
      for (int e = 0; e < 4; ++e) mx = __builtin_elementwise_max(mx, __builtin_bit_cast(us2, tv[it][e] & 0x7fff7fffu));
    }
    if (w == 0) *(unsigned*)(smem + l * 528 + 512) = tv[4][0];
    const unsigned wm = wavemax_bits((unsigned)max(mx[0], mx[1]) << 16);
    if (l == 0) red[w] = wm;
  }
  u64 m0 = __hip_atomic_load(mg, RLX), m1 = __hip_atomic_load(mg + 32, RLX);
  bar();
  STAMP(8);
  unsigned be = 0;
#pragma unroll
  for (int i = 0; i < 8; ++i) be = max(be, red[i]);
  be = __builtin_amdgcn_readfirstlane(scale_exp(be));
  const unsigned bk = asU(scale_up(be));
  if (tid == 64) __hip_atomic_store((gu64*)(ws + OFF_BETA + k1 * 8), ((u64)tag << 32) | bk, RLX);
  u4 wv[4], yv[4];
  filter_gather(smem, k1, tid, scale_inv(be), wv);
  asm volatile("" : "+v"(wv[0]), "+v"(wv[1]), "+v"(wv[2]), "+v"(wv[3]));
  STAMP(1);
  {
    gu32* fa = (gu32*)(ws + OFF_FA + l * FS);
    for (int spin = 0; spin < SPIN_CAP; ++spin) {
      bool ok = true;
      if (w == 0) ok = (__hip_atomic_load(fa, RLX) == tag) & (__hip_atomic_load(fa + 16 * FS, RLX) == tag);
      const bool mok = __all((m0 >> 32) == tag && (m1 >> 32) == tag);
      if (!mok) {
        m0 = __hip_atomic_load(mg, RLX);
        m1 = __hip_atomic_load(mg + 32, RLX);
      }
      if (__all(ok && (m0 >> 32) == tag && (m1 >> 32) == tag)) break;
      __builtin_amdgcn_s_sleep(1);
    }
    const unsigned am = wavemax_bits(max((unsigned)m0 & 0x7fffffffu, (unsigned)m1 & 0x7fffffffu));
    if (l == 0) red[8 + w] = am;
  }
  bar();
  STAMP(3);
#pragma unroll
  for (int i = 0; i < 4; ++i) yv[i] = ld16(ws, OFF_Y + slot * 32768 + (4 * i + g) * 2048 + w * 256 + i16 * 16);
  u64 bg0 = __hip_atomic_load((gu64*)(ws + OFF_BETA + l * 8), RLX);
  u64 bg1 = __hip_atomic_load((gu64*)(ws + OFF_BETA + 512), RLX);
  __builtin_amdgcn_sched_barrier(0);
  unsigned ae = 0;
#pragma unroll
  for (int i = 0; i < 8; ++i) ae = max(ae, red[8 + i]);
  ae = scale_exp(ae);
  const f2 mraw = {asF((unsigned)m0), asF((unsigned)m1)};
  {
    const float sc = scale_inv(ae) * (w < 4 ? 11.313708498984761f : 1.f);
    *(unsigned*)(smem + P1_MOD + (w >> 2) * 1024 + n32 * 32 + ((tid >> 5) & 7) * 4) = pkh(mraw[0] * sc, mraw[1] * sc);
  }
  bar();
  __builtin_amdgcn_sched_barrier(0);
  h8 mv[4], f32t[16];
  const char* mp = smem + P1_MOD + w * 128 + (g & 1) * 16;
#pragma unroll
  for (int t = 0; t < 4; ++t) mv[t] = *(const h8*)(mp + 1024 + t * 32);
#pragma unroll
  for (int i = 0; i < 16; ++i) f32t[i] = *(const h8*)(tab + i * 1024);
  const h8 f16re = *(const h8*)(tab + 16 * 1024), f16im = *(const h8*)(tab + 17 * 1024);
  const h8 gre = *(const h8*)(tab + 18 * 1024), gim = *(const h8*)(tab + 19 * 1024);
  const h8 ghre = *(const h8*)(tab + 20 * 1024), ghim = *(const h8*)(tab + 21 * 1024);
  h8 if16[2] = {*(const h8*)(tab + 22 * 1024), *(const h8*)(tab + 23 * 1024)};
  __builtin_amdgcn_sched_barrier(0);

  const f4 z = {0.f, 0.f, 0.f, 0.f};
  {
    char* yb = T2 + w * 4096;
#pragma unroll
    for (int i = 0; i < 4; ++i) *(u4*)(yb + (4 * i + g) * 256 + ((i16 ^ (4 * i + g)) * 16)) = yv[i];
    const int go = realonly ? ((g & 1) + (k1 >> 6) * 2) : g;
    h8 a[4];
#pragma unroll
    for (int t = 0; t < 4; ++t) a[t] = *(const h8*)(yb + i16 * 256 + (((4 * t + go) ^ i16) * 16)) * mv[t];
    if (realonly) {
#pragma unroll
      for (int t = 0; t < 4; ++t) {
        if (k1 == 0) a[t] += *(const h8*)(mp + t * 32);
        if (g >= 2) a[t] = asH(u4{0u, 0u, 0u, 0u});
      }
    }
    __builtin_amdgcn_sched_barrier(0);
    STAMP(4);
    f4 dr[4], di[4];
#pragma unroll
    for (int t = 0; t < 4; ++t) { dr[t] = mm16(a[t], f16re, z); di[t] = mm16(a[t], f16im, z); }
    __builtin_amdgcn_sched_barrier(0);
#pragma unroll
    for (int t = 0; t < 4; ++t) {
      const h8 xb = pack8(dr[t], di[t]);
      dr[t] = mm16(gre, xb, z);
      di[t] = mm16(gim, xb, z);
    }
    __builtin_amdgcn_sched_barrier(0);
    f2 pr[4][2], pi[4][2];
#pragma unroll
    for (int t = 0; t < 4; ++t) {
      pr[t][0] = lo2(dr[t]); pr[t][1] = hi2(dr[t]); pi[t][0] = lo2(di[t]); pi[t][1] = hi2(di[t]);
      cmulc(pr[t][0], pi[t][0], tc1[t], ts1[t]);
      cmulc(pr[t][1], pi[t][1], tc1[t], ts1[t]);
    }
#pragma unroll
    for (int e = 0; e < 4; ++e) {
      const int k2 = 4 * g + e;
      const u4 o = {pkh(pr[0][e >> 1][e & 1], pr[1][e >> 1][e & 1]), pkh(pr[2][e >> 1][e & 1], pr[3][e >> 1][e & 1]),
                    pkh(pi[0][e >> 1][e & 1], pi[1][e >> 1][e & 1]), pkh(pi[2][e >> 1][e & 1], pi[3][e >> 1][e & 1])};
      *(u4*)(T1 + i16 * 2064 + k2 * 128 + ((w ^ ((k2 >> 1) & 7)) * 16)) = o;
    }
  }
  bar();
  STAMP(5);

  {
    const char* tb = T1 + (2 * w + ro) * 2064 + i16 * 128;
    h8 bq[4];
#pragma unroll
    for (int kk = 0; kk < 4; ++kk) bq[kk] = *(const h8*)(tb + (((2 * kk + h) ^ ((i16 >> 1) & 7)) * 16));
    f16v xr = zero16(), xi = zero16();
#pragma unroll
    for (int kk = 0; kk < 4; ++kk) {
      xr = mm32h(f32t[4 * kk], bq[kk], xr);
      xi = mm32h(f32t[4 * kk + 1], bq[kk], xi);
    }
    __builtin_amdgcn_sched_barrier(0);
    f16v ur = zero16(), ui = zero16();
#pragma unroll
    for (int q = 0; q < 4; ++q) {
      const h8 wq = asH(wv[q]);
      const h4 r4 = __builtin_bit_cast(h4, u2{pkh(xr[4 * q], xr[4 * q + 1]), pkh(xr[4 * q + 2], xr[4 * q + 3])});
      const h4 i4 = __builtin_bit_cast(h4, u2{pkh(xi[4 * q], xi[4 * q + 1]), pkh(xi[4 * q + 2], xi[4 * q + 3])});
      const h4 wr4 = __builtin_shufflevector(wq, wq, 0, 1, 2, 3), wi4 = __builtin_shufflevector(wq, wq, 4, 5, 6, 7);
      const h4 zr = __builtin_elementwise_fma(r4, wr4, -(i4 * wi4)), zi = __builtin_elementwise_fma(r4, wi4, i4 * wr4);
      const h8 za = __builtin_shufflevector(zr, zi, 0, 1, 2, 3, 4, 5, 6, 7);
      ur = mm32h(za, f32t[4 * q + 2], ur);
      ui = mm32h(za, f32t[4 * q + 3], ui);
      __builtin_amdgcn_sched_barrier(0);
    }
    f16v v[2];
#pragma unroll
    for (int t = 0; t < 2; ++t) {
      const h8 br = frag4(pkh(ur[8 * t], ur[8 * t + 1]), pkh(ur[8 * t + 2], ur[8 * t + 3]), pkh(ur[8 * t + 4], ur[8 * t + 5]), pkh(ur[8 * t + 6], ur[8 * t + 7]));
      const h8 bi = frag4(pkh(ui[8 * t], ui[8 * t + 1]), pkh(ui[8 * t + 2], ui[8 * t + 3]), pkh(ui[8 * t + 4], ui[8 * t + 5]), pkh(ui[8 * t + 6], ui[8 * t + 7]));
      v[t] = mm32h(ghim, bi, mm32h(ghre, br, zero16()));
      __builtin_amdgcn_sched_barrier(0);
    }
    f2 qr[2][4], qi[2][4];
#pragma unroll
    for (int t = 0; t < 2; ++t)
#pragma unroll
      for (int i = 0; i < 4; ++i) {
        qr[t][i] = f2{v[t][2 * i], v[t][2 * i + 1]};
        qi[t][i] = f2{v[t][8 + 2 * i], v[t][8 + 2 * i + 1]};
        cmulc(qr[t][i], qi[t][i], tc2[t], ts2[t]);
      }
    char* wb = T2 + (4 * h * 32 + n32) * 64 + (((w >> 1) ^ ((n32 >> 2) & 3)) * 16) + (w & 1) * 8;
#pragma unroll
    for (int e = 0; e < 8; ++e)
      *(u2*)(wb + (8 * (e >> 2) + (e & 3)) * 2048) = u2{pkh(qr[0][e >> 1][e & 1], qr[1][e >> 1][e & 1]), pkh(qi[0][e >> 1][e & 1], qi[1][e >> 1][e & 1])};
  }
  STAMP(2);
  {
    for (int spin = 0; spin < SPIN_CAP && !__all((bg0 >> 32) == tag && (bg1 >> 32) == tag); ++spin) {
      bg0 = __hip_atomic_load((gu64*)(ws + OFF_BETA + l * 8), RLX);
      bg1 = __hip_atomic_load((gu64*)(ws + OFF_BETA + 512), RLX);
    }
    const unsigned bmax = wavemax_bits(max((unsigned)bg0, (unsigned)bg1));
    const unsigned de = bmax - bk;
    const _Float16 ratio = (_Float16)asF(de < 0x3f800000u ? 0x3f800000u - de : 0u);
    if16[0] *= ratio;
    if16[1] *= ratio;
    if (k1 == 0 && tid == 0) st4(ws, OFF_YSC, asU(asF(bmax) * scale_up(ae)));
  }
  bar();
  STAMP(6);

  const int krow = realonly ? (k1 >> 6) : (2 * slot + ro);
  f16v acc[2];
#pragma unroll
  for (int t = 0; t < 2; ++t) {
    const char* tb = T2 + ((2 * w + t) * 32 + n32) * 64;
    acc[t] = zero16();
#pragma unroll
    for (int st = 0; st < 2; ++st) acc[t] = mm32h(*(const h8*)(tb + (((2 * st + h) ^ ((n32 >> 2) & 3)) * 16)), if16[st], acc[t]);
  }
#pragma unroll
  for (int t = 0; t < 2; ++t)
#pragma unroll
    for (int a = 0; a < 2; ++a) {
      const auto r0 = __builtin_amdgcn_permlane32_swap(pkh(acc[t][8 * a], acc[t][8 * a + 1]), pkh(acc[t][8 * a + 4], acc[t][8 * a + 5]), false, false);
      const auto r1 = __builtin_amdgcn_permlane32_swap(pkh(acc[t][8 * a + 2], acc[t][8 * a + 3]), pkh(acc[t][8 * a + 6], acc[t][8 * a + 7]), false, false);
      const unsigned off = OFF_V + ((2 * w + t) * 128 + krow) * 1024 + (2 * a + h) * 256 + d1 * 16;
      st16(ws, (realonly && ro) ? 0x7fffff00u : off, u4{r0[0], r1[0], r0[1], r1[1]});
    }
}

constexpr int SLOT = 12288, NSLOT = 13, RING = SLOT * NSLOT, NPRE = 11, LDS_BYTES = 163840;
constexpr int P2_IMG = NPRE * SLOT;

DI void glds_v(const char* p, unsigned lds) {
  asm volatile("s_mov_b32 m0, %1\n\ts_nop 0\n\tglobal_load_lds_dwordx4 %0, off" :: "v"(p), "s"(lds) : "memory");
}
DI void glds_s(unsigned voff, const char* base, unsigned lds) {
  asm volatile("s_mov_b32 m0, %2\n\ts_nop 4\n\tglobal_load_lds_dwordx4 %0, %1" :: "v"(voff), "s"(base), "s"(lds) : "memory");
}
DI void ld1(u4& a, unsigned voff, const char* base) {
  asm volatile("global_load_dwordx4 %0, %1, %2 sc1" : "=v"(a) : "v"(voff), "s"(base) : "memory");
}
struct Loader {
  const char* p1[3];
  const char* w2;
  unsigned lds, voff2;
  int lw;
};
DI void load_w1(const Loader& L, int s) {
#pragma unroll
  for (int j = 0; j < 3; ++j) glds_v(L.p1[j] + s * (32 * FH * 2), L.lds + (s % NSLOT) * SLOT + j * 1024);
}
DI int w2_step(int r) { return r + 2 * ((r >= 20) + (r >= 40) + (r >= 59)); }
DI void load_w2(const Loader& L, int p) {
#pragma unroll
  for (int j = 0; j < 3; ++j) {
    const int hs = 12 * p + 3 * L.lw + j;
    glds_s(L.voff2, L.w2 + (w2_step(hs >> 1) * 16 + (hs & 1) * 8) * 1024, L.lds + ((p + 4) % NSLOT) * SLOT + j * 1024);
  }
}

constexpr int SYNC = RING + 1024;
DI void prog_set(char* smem, int w, unsigned v) { *(volatile LDS3 unsigned*)(smem + SYNC + w * 4) = v; }
DI void prog_wait(char* smem, unsigned n, unsigned& seen) {
  for (int spin = 0; seen < n && spin < (SPIN_CAP << 4); ++spin) {
    const u4 v = *(volatile LDS3 u4*)(smem + SYNC);
    seen = __builtin_amdgcn_readfirstlane(min(min(v[0], v[1]), min(v[2], v[3])));
    if (seen < n) __builtin_amdgcn_s_sleep(1);
  }
  asm volatile("" ::: "memory");
}
DI void lds_arrive(char* smem, int l) {
  if (l == 0) __hip_atomic_fetch_add((LDS3 unsigned*)(smem + SYNC + 16), 1u, __ATOMIC_RELAXED, __HIP_MEMORY_SCOPE_WORKGROUP);
}
DI unsigned lds_peek(char* smem) { return *(volatile LDS3 unsigned*)(smem + SYNC + 16); }
template <int N> DI void lds_wait(char* smem, unsigned v) {
  unsigned sv, cnt = SPIN_CAP << 4;
  asm volatile("s_nop 0\n1:\n\tv_readfirstlane_b32 %1, %0\n\ts_cmp_ge_u32 %1, %4\n\ts_cbranch_scc1 2f\n\tds_read_b32 %0, %3\n\t"
               "s_waitcnt lgkmcnt(0)\n\ts_sub_u32 %2, %2, 1\n\ts_cbranch_scc0 1b\n2:"
               : "+v"(v), "=&s"(sv), "+s"(cnt) : "v"((unsigned)(size_t)(LDS3 char*)(smem + SYNC + 16)), "i"(N) : "memory", "scc");
}

struct P2Pre { u4 xv[2], bfr[8]; };
DI void phase2_pre(const u16* __restrict__ x, P2Pre& P, int s2, int c8, int w, int l) {
  const int n32 = l & 31, h = l >> 5, st = w & 3, dh = w >> 2;
  const int s1 = 32 * st + n32;
  const int d0 = 32 * (8 * (c8 & 1) + 4 * dh + h) + 8 * (c8 >> 1);
  P.xv[0] = *(const u4*)(x + (size_t)(16 * s1 + s2) * 512 + d0);
  P.xv[1] = *(const u4*)(x + (size_t)(16 * s1 + s2) * 512 + d0 + 64);
  float cv[4], sv[4];
#pragma unroll
  for (int e = 0; e < 4; ++e) {
    const float r = (float)((s1 * (4 * h + e)) & 127) * (1.f / 128.f);
    cv[e] = 0.1767766952966369f * cosr(r);
    sv[e] = -0.1767766952966369f * sinr(r);
  }
  const float rr = (float)(s1 & 15) * 0.0625f, rc = cosr(rr), rsn = sinr(rr);
#pragma unroll
  for (int ks = 0; ks < 8; ++ks) {
#pragma unroll
    for (int e = 0; e < 4; ++e) {
      P.bfr[ks][e] = pkh(cv[e], sv[e]);
      if (ks < 7) {
        const float cn = cv[e] * rc + sv[e] * rsn;
        sv[e] = sv[e] * rc - cv[e] * rsn;
        cv[e] = cn;
      }
    }
  }
  if (h == 0) P.bfr[0][0] = pkh(0.08838834764831845f, (s1 & 1) ? -0.08838834764831845f : 0.08838834764831845f);
  asm volatile("" : "+v"(P.bfr[0]), "+v"(P.bfr[1]), "+v"(P.bfr[2]), "+v"(P.bfr[3]), "+v"(P.bfr[4]), "+v"(P.bfr[5]), "+v"(P.bfr[6]),
               "+v"(P.bfr[7]), "+v"(P.xv[0]), "+v"(P.xv[1]));
}

constexpr int P2_MOD = RING;
DI void phase2(const P2Pre& P, char* ws, char* smem, unsigned tag, int s2, int c8, int w, int l) {
  const int tid = w * 64 + l, n32 = l & 31, h = l >> 5, st = w & 3, dh = w >> 2;
  const int s1 = 32 * st + n32;
  const int d0 = 32 * (8 * (c8 & 1) + 4 * dh + h) + 8 * (c8 >> 1);
  f4 ga[4], cb[4];
#pragma unroll
  for (int i = 0; i < 4; ++i) {
    const char* p = smem + P2_MOD + (4 * dh + h + 2 * (i >> 1)) * 32 + (i & 1) * 16;
    ga[i] = *(const f4*)p;
    cb[i] = *(const f4*)(p + 256);
  }
  STAMP(8);
  await(ws, OFF_FB, 65, tag, w, l);
  STAMP(9);
  char* img = smem + P2_IMG;
  const unsigned vo = OFF_V + (s2 * 128 + (tid >> 3)) * 1024 + c8 * 128 + (tid & 7) * 16;
  const u4 v0 = ld16(ws, vo), v1 = ld16(ws, vo + 65536);
  const float ysc = asF(ld4(ws, OFF_YSC));
  {
    const int o = (tid * 16) ^ (((tid >> 4) & 1) * 64);
    *(u4*)(img + o) = v0;
    *(u4*)(img + 8192 + o) = v1;
  }
#pragma unroll
  for (int i = 0; i < 4; ++i) ga[i] *= ysc;
  bar();
  STAMP(10);
  const int q = (l & 15) >> 2, p = l & 3, gi = (l >> 4) & 1;
  const char* a = img + (8 * h + q) * 128 + (((32 * dh + 16 * gi + 8 * (p & 1) + 4 * (p >> 1)) * 2) ^ ((q >> 1) * 64));
  f16v acc = zero16();
  {
    h8 af[8];
#pragma unroll
    for (int ks = 0; ks < 8; ++ks) af[ks] = trfrag(a + ks * 2048, a + ks * 2048 + 512);
#pragma unroll
    for (int ks = 0; ks < 8; ++ks) acc = mm32h(af[ks], asH(P.bfr[ks]), acc);
  }
  const unsigned fo = ((s2 * 4 + st) * 32 + (d0 >> 4)) * 1024 + (n32 + 32 * ((d0 >> 3) & 1)) * 16;
  float sm = 0.f, sq = 0.f;
  u4 xo[2];
#pragma unroll
  for (int t = 0; t < 2; ++t) {
    u4 &o = xo[t], os;
#pragma unroll
    for (int e = 0; e < 4; ++e) {
      const f4 gv = ga[2 * t + (e >> 1)], cv = cb[2 * t + (e >> 1)];
      const unsigned xx = P.xv[t][e];
      const unsigned r = pkbf(fmaf(acc[8 * t + 2 * e], gv[(2 * e) & 3], bflo(xx)), fmaf(acc[8 * t + 2 * e + 1], gv[(2 * e + 1) & 3], bfhi(xx)));
      o[e] = r;
      const float a0 = bflo(r), a1 = bfhi(r);
      os[e] = pkbf(a0 * cv[(2 * e) & 3], a1 * cv[(2 * e + 1) & 3]);
      sm += a0 + a1;
      sq = fmaf(a0, a0, fmaf(a1, a1, sq));
    }
    st16(ws, OFF_XS + fo + t * 4096, os);
  }
  const auto e0 = __builtin_amdgcn_permlane32_swap(asU(sm), asU(sq), false, false);
  st4(ws, OFF_ST + ((s2 * 16 + 2 * c8 + dh) * 128 + s1) * 8 + h * 4, asU(asF(e0[0]) + asF(e0[1])));
  publish(ws, OFF_FC + (s2 * 8 + c8) * FS, tag);
  st16(ws, OFF_X1 + fo, xo[0]);
  st16(ws, OFF_X1 + fo + 4096, xo[1]);
}

#define ACTA 10
constexpr int ACT_AHEAD = ACTA;
template <int N> DI void wait1(u4& a) { asm volatile("s_waitcnt vmcnt(%1)" : "+v"(a) : "i"(N) : "memory"); }

struct G1Ctx { const char *se[3], *so[3], *ab; char* smem; unsigned voff; int w; };
template <int K, int N> DI h8 g1_frag(const G1Ctx& cx) {
  constexpr int O = ((K / 2) % NSLOT) * SLOT + (K & 1) * 6144, W = O / 61440;
  const char* p = ((N & 1) ? cx.so[W] : cx.se[W]) + (O - W * 61440) + N * 32;
  return trfrag(p, p + 4 * 384);
}
DI void h_store(char* ws, unsigned off, const f16v& a, float rs2) {
  u4 o;
#pragma unroll
  for (int e = 0; e < 4; ++e) {
    const int i0 = 8 * (e >> 1) + 2 * (e & 1);
    o[e] = pkbf((a[i0] * rs2) * a[i0 + 4], (a[i0 + 1] * rs2) * a[i0 + 5]);
  }
  st16(ws, off, o);
}
#define TAIL 8
template <int NT> constexpr int unit_k(int u) {
  constexpr int H = NT / 2, U0 = (32 - TAIL) * NT;
  return u < U0 ? u / NT : 32 - TAIL + ((u - U0) % (TAIL * H)) / H;
}
template <int NT> constexpr int unit_n(int u) {
  constexpr int H = NT / 2, U0 = (32 - TAIL) * NT;
  return u < U0 ? u % NT : (u - U0) % H + H * ((u - U0) / (TAIL * H));
}
template <int NT> constexpr int stages_done(int u) {
  int s = 0;
  for (; s < 16; ++s) {
    bool open = false;
    for (int v = u + 1; v < 32 * NT; ++v) open |= unit_k<NT>(v) / 2 == s;
    if (open) break;
  }
  return s;
}
template <int NT, int U, class Mid>
DI void g1_unit(f16v (&acc)[NT], u4 (&act)[32], h8 (&fr)[NT], const G1Ctx& cx, Mid& mid, char* ws, unsigned hb, const float& rs2, unsigned& peek) {
  constexpr int K = unit_k<NT>(U), N = unit_n<NT>(U), X = U + NT, U1 = 32 * NT - TAIL * (NT / 2);
  if constexpr (N == 0) {
    if constexpr (K > 0) wait1<(K + ACT_AHEAD <= 32 ? ACT_AHEAD - 1 : 31 - K)>(act[K]);
    if constexpr (K == 1) STAMP(13);
    if constexpr (K == 16) STAMP(14);
  }
  constexpr int XA = 2 * NPRE * NT, XB = (32 - TAIL) * NT + (2 * NSLOT - 32 + TAIL) * (NT / 2);
  static_assert(2 * NPRE <= 32 - TAIL && 2 * NSLOT >= 32 - TAIL, "stage checks assume this order");
  if constexpr (X == XA) lds_wait<4>(cx.smem, peek);
  if constexpr (X == XB) lds_wait<8>(cx.smem, peek);
  acc[N] = mm32b(__builtin_bit_cast(b8, fr[U % NT]), asB(act[K]), acc[N]);
  __builtin_amdgcn_sched_barrier(0);
  if constexpr (X < 32 * NT) fr[U % NT] = g1_frag<unit_k<NT>(X < 32 * NT ? X : 0), unit_n<NT>(X < 32 * NT ? X : 0)>(cx);
  if constexpr (N == 1 % (NT / 2) && U < U1 && K + ACT_AHEAD < 32) ld1(act[K + ACT_AHEAD], cx.voff, cx.ab + (K + ACT_AHEAD) * 1024);
  if constexpr (X + NT == XA || X + NT == XB) peek = lds_peek(cx.smem);
  if constexpr (U >= 5 * NT && U < 5 * NT + 4 + NT) mid.template operator()<U - 5 * NT>();
  if constexpr (U >= U1 + 2 && (U - U1 - 2) % 4 == 0 && (U - U1 - 2) / 4 < NT / 2) h_store(ws, hb + (U - U1 - 2) / 4 * 1024, acc[(U - U1 - 2) / 4], rs2);
  __builtin_amdgcn_sched_barrier(0);
  constexpr int SD = stages_done<NT>(X);
  if constexpr (X < 32 * NT && SD > stages_done<NT>(X - 1)) prog_set(cx.smem, cx.w, SD);
  if constexpr (U + 1 < 32 * NT) g1_unit<NT, U + 1>(acc, act, fr, cx, mid, ws, hb, rs2, peek);
}

template <int NT> DI void g1_compute(char* ws, char* smem, unsigned tag, int s2, int chunk, int w, int l) {
  const int h = l >> 5, q = (l & 15) >> 2, p = l & 3;
  G1Ctx cx;
  cx.voff = l * 16;
  cx.smem = smem;
  cx.w = w;
  cx.ab = ws + OFF_XS + (s2 * 4 + w) * 32768;
  {
    const char* b0 = smem + (8 * h + q) * 384 + (p >> 1) * 192 + (p & 1) * 16 + ((l >> 4) & 1) * 8;
#pragma unroll
    for (int i = 0; i < 3; ++i) {
      unsigned e = (unsigned)(size_t)(LDS3 const char*)(b0 + (q >> 1) * 32 + i * 61440), o = e - (q >> 1) * 64;
      asm volatile("" : "+v"(e), "+v"(o));
      cx.se[i] = (const char*)(LDS3 const char*)(size_t)e;
      cx.so[i] = (const char*)(LDS3 const char*)(size_t)o;
    }
  }
  h8 fr[NT];
  if (w == 0) poll<FS>(ws, OFF_FC + s2 * 8 * FS, 8, tag, l);
  waitv<0>();
  bar();
  STAMP(20);
  u2 stv[8];
  u4 act[32];
  asm volatile("s_nop 4" ::: "memory");
  ld1(act[0], cx.voff, cx.ab);
  ld1(act[1], cx.voff, cx.ab + 1024);
  {
    const char* sb = ws + OFF_ST + s2 * 16384 + w * 256;
    const unsigned so = (l & 31) * 8 + h * 8192;
    asm volatile("s_nop 4\n\tglobal_load_dwordx2 %0, %8, %9 sc1\n\tglobal_load_dwordx2 %1, %8, %9 offset:1024 sc1\n\t"
                 "global_load_dwordx2 %2, %8, %9 offset:2048 sc1\n\tglobal_load_dwordx2 %3, %8, %9 offset:3072 sc1\n\t"
                 "global_load_dwordx2 %4, %8, %10 sc1\n\tglobal_load_dwordx2 %5, %8, %10 offset:1024 sc1\n\t"
                 "global_load_dwordx2 %6, %8, %10 offset:2048 sc1\n\tglobal_load_dwordx2 %7, %8, %10 offset:3072 sc1"
                 : "=&v"(stv[0]), "=&v"(stv[1]), "=&v"(stv[2]), "=&v"(stv[3]), "=&v"(stv[4]), "=&v"(stv[5]), "=&v"(stv[6]), "=&v"(stv[7])
                 : "v"(so), "s"(sb), "s"(sb + 4096) : "memory");
  }
  u2 pq[6];
  {
    const char* pb = ws + __builtin_amdgcn_readfirstlane(OFF_PQ + chunk * 1536);
    const unsigned po = (l & 31) * 8;
    asm volatile("s_nop 4\n\tglobal_load_dwordx2 %0, %6, %7 sc1\n\tglobal_load_dwordx2 %1, %6, %7 offset:256 sc1\n\t"
                 "global_load_dwordx2 %2, %6, %7 offset:512 sc1\n\tglobal_load_dwordx2 %3, %6, %7 offset:768 sc1\n\t"
                 "global_load_dwordx2 %4, %6, %7 offset:1024 sc1\n\tglobal_load_dwordx2 %5, %6, %7 offset:1280 sc1"
                 : "=&v"(pq[0]), "=&v"(pq[1]), "=&v"(pq[2]), "=&v"(pq[3]), "=&v"(pq[4]), "=&v"(pq[5]) : "v"(po), "s"(pb) : "memory");
  }
#pragma unroll
  for (int i = 2; i < ACT_AHEAD; ++i) ld1(act[i], cx.voff, cx.ab + i * 1024);
#pragma unroll
  for (int n = 0; n < NT; ++n) {
    const char* a = ((n & 1) ? cx.so[0] : cx.se[0]) + n * 32;
    fr[n] = trfrag(a, a + 4 * 384);
  }
  f16v acc[NT];
#pragma unroll
  for (int n = 0; n < NT; ++n) acc[n] = zero16();
  float rs2 = 0.f;
  f2 sv;
  float mean, sd;
  s4 bx;
  auto mid = [&]<int P>() {
    if constexpr (P == 0) {
      asm volatile("" : "+v"(stv[0]), "+v"(stv[1]), "+v"(stv[2]), "+v"(stv[3]), "+v"(stv[4]), "+v"(stv[5]), "+v"(stv[6]), "+v"(stv[7]));
      asm volatile("" : "+v"(pq[0]), "+v"(pq[1]), "+v"(pq[2]), "+v"(pq[3]), "+v"(pq[4]), "+v"(pq[5]));
      sv = __builtin_bit_cast(f2, stv[0]);
#pragma unroll
      for (int i = 1; i < 8; ++i) sv += __builtin_bit_cast(f2, stv[i]);
      asm volatile("" : "+v"(sv));
    } else if constexpr (P == 1) {
      const auto e0 = __builtin_amdgcn_permlane32_swap(asU(sv[0]), asU(sv[0]), false, false);
      const auto e1 = __builtin_amdgcn_permlane32_swap(asU(sv[1]), asU(sv[1]), false, false);
      mean = (asF(e0[0]) + asF(e0[1])) * (1.f / 512.f);
      sd = fmaxf((asF(e1[0]) + asF(e1[1])) * (1.f / 512.f) - mean * mean, 0.f);
      asm volatile("" : "+v"(mean), "+v"(sd));
    } else if constexpr (P == 2) {
      sd = rbf(__builtin_amdgcn_sqrtf(rbf(rbf(sd) + 1e-5f)));
      asm volatile("" : "+v"(sd));
    } else if constexpr (P == 3) {
      rs2 = __builtin_amdgcn_rcpf(sd);
      rs2 *= rs2;
      bx = __builtin_bit_cast(s4, h ? u2{0u, 0u} : u2{pkbf(-mean, -mean), pkbf(sd, sd)});
      asm volatile("" : "+v"(rs2), "+v"(bx));
    } else {
      acc[P - 4] = __builtin_amdgcn_mfma_f32_32x32x8bf16_1k(__builtin_bit_cast(s4, pq[P - 4]), bx, acc[P - 4], 0, 0, 0);
    }
  };
  __builtin_amdgcn_sched_barrier(0);
  wait1<ACT_AHEAD + 13>(act[0]);
  const unsigned hb = OFF_H + ((s2 * 4 + w) * 86 + chunk * 6) * 1024 + l * 16;
  unsigned peek = 0;
  g1_unit<NT, 0>(acc, act, fr, cx, mid, ws, hb, rs2, peek);
  STAMP(15);
#pragma unroll
  for (int n = NT / 2; n < NT; ++n) h_store(ws, hb + n * 1024, acc[n], rs2);
  waitv<0>();
}

template <int S> DI void g1_load_step(const Loader& L, char* smem, int l, unsigned& seen) {
  prog_wait(smem, S + 1, seen);
  if constexpr (S <= 2) load_w1(L, S + 13);
  else load_w2(L, (S + 9) % NSLOT);
  if constexpr (S == 2) { waitv<9>(); lds_arrive(smem, l); }
  if constexpr (S == 5) { waitv<9>(); lds_arrive(smem, l); }
  if constexpr (S < 15) g1_load_step<S + 1>(L, smem, l, seen);
}

DI void w2_frags(const char* p, int sw, u4& w0, u4& w1) {
  w0 = __builtin_bit_cast(u4, trfrag(p + sw, p + sw + 512));
  w1 = __builtin_bit_cast(u4, trfrag(p + (64 - sw), p + (64 - sw) + 512));
}
DI int w2_lane(int l) { return (8 * (l >> 5) + ((l & 15) >> 2)) * 128 + ((l >> 4) & 1) * 32 + (l & 1) * 16 + ((l >> 1) & 1) * 8; }
#define HWIN 5
constexpr int G2_AHEAD = 4;
DI int w2_off(int r) { const int o = r * 2048 + 4 * SLOT; return o >= RING ? o - RING : o; }
template <int I> DI void g2_step(f16v (&acc)[2], u4 (&hf)[22], u4 (&wf)[G2_AHEAD][2], const char* base, int sw, int r0, bool shortq,
                                 const char* hb, const char* hd, unsigned voff) {
  wait1<(I + HWIN <= 22 ? HWIN - 1 : 21 - I)>(hf[I]);
  if (I < 19 || !shortq) {
    acc[0] = mm32b(asB(wf[I % G2_AHEAD][0]), asB(hf[I]), acc[0]);
    acc[1] = mm32b(asB(wf[I % G2_AHEAD][1]), asB(hf[I]), acc[1]);
  }
  __builtin_amdgcn_sched_barrier(0);
  if constexpr (I + HWIN < 22) ld1(hf[I + HWIN], voff, (I + HWIN < 20 ? hb : hd) + (I + HWIN) * 1024);
  if constexpr (I + G2_AHEAD < 20) w2_frags(base + w2_off(r0 + I + G2_AHEAD), sw, wf[I % G2_AHEAD][0], wf[I % G2_AHEAD][1]);
  __builtin_amdgcn_sched_barrier(0);
  if constexpr (I < 19) g2_step<I + 1>(acc, hf, wf, base, sw, r0, shortq, hb, hd, voff);
}

DI void gemm2(char* ws, char* smem, u16* __restrict__ out, const u4 (&wd)[4], unsigned tag, int rt, int ct, int w, int l) {
  const int a = w & 1, kq = ((w >> 1) + ct) & 3, n32 = l & 31, h = l >> 5;
  const unsigned voff = l * 16;
  const bool shortq = kq >= 2;
  const int ks0 = kq < 2 ? 22 * kq : 21 * kq + 2, r0 = ks0 - 2 * kq;
  const int row = (w * 64 + l) >> 3, oct = l & 7, dcol = 64 * ct + 8 * oct;
  waitv<0>();
  STAMP(17);
  bar();
  poll<FS>(ws, OFF_FD + ((rt >> 1) * 16 + ks0 / 6) * FS, kq == 3 ? 5 : (kq == 0 ? 4 : 5 - (kq >> 1)), tag, l);
  STAMP(18);
  u4 hf[22], wf[G2_AHEAD][2];
  const char* hb = ws + OFF_H + ((2 * rt + a) * 86 + ks0) * 1024;
  const char* hd = hb - (shortq ? 1024 : 0);
  u4 xr;
  f4 g0, g1;
  {
    const unsigned xo = OFF_X1 + ((2 * rt + (row >> 5)) * 32 + (dcol >> 4)) * 1024 + ((row & 31) + 32 * (oct & 1)) * 16;
    const unsigned go = OFF_MOD + (2560 + dcol) * 4;
    asm volatile("s_nop 4\n\tglobal_load_dwordx4 %0, %3, %5 sc1\n\tglobal_load_dwordx4 %1, %4, %5 sc1\n\tglobal_load_dwordx4 %2, %4, %5 offset:16 sc1"
                 : "=&v"(xr), "=&v"(g0), "=&v"(g1) : "v"(xo), "v"(go), "s"(ws) : "memory");
  }
#pragma unroll
  for (int i = 0; i < HWIN; ++i) ld1(hf[i], voff, (i < 20 ? hb : hd) + i * 1024);
#pragma unroll
  for (int i = 0; i < G2_AHEAD; ++i) w2_frags(smem + w2_lane(l) + w2_off(r0 + i), (l & 8) * 8, wf[i][0], wf[i][1]);
  __builtin_amdgcn_sched_barrier(0);
  STAMP(22);
  f16v acc[2] = {zero16(), zero16()};
  g2_step<0>(acc, hf, wf, smem + w2_lane(l), (l & 8) * 8, r0, shortq, hb, hd, voff);
  wait1<1>(hf[20]);
  acc[0] = mm32b(asB(wd[0]), asB(hf[20]), acc[0]);
  acc[1] = mm32b(asB(wd[1]), asB(hf[20]), acc[1]);
  asm volatile("s_waitcnt vmcnt(0)" : "+v"(hf[21]), "+v"(xr), "+v"(g0), "+v"(g1) :: "memory");
  acc[0] = mm32b(asB(wd[2]), asB(hf[21]), acc[0]);
  acc[1] = mm32b(asB(wd[3]), asB(hf[21]), acc[1]);
  STAMP(19);
  bar();
  float* red = (float*)smem;
#pragma unroll
  for (int c = 0; c < 2; ++c)
#pragma unroll
    for (int qd = 0; qd < 4; ++qd)
      *(f4*)(red + (kq * 64 + 32 * a + n32) * 68 + 32 * c + 16 * (qd >> 1) + 8 * h + 4 * (qd & 1)) =
          f4{acc[c][4 * qd], acc[c][4 * qd + 1], acc[c][4 * qd + 2], acc[c][4 * qd + 3]};
  bar();
  f4 s0 = {0.f, 0.f, 0.f, 0.f}, s1 = {0.f, 0.f, 0.f, 0.f};
#pragma unroll
  for (int i = 0; i < 4; ++i) {
    const float* p = red + (i * 64 + row) * 68 + 8 * oct;
    s0 += *(const f4*)p;
    s1 += *(const f4*)(p + 4);
  }
  const u4 o = {pkbf(fmaf(s0[0], g0[0], bflo(xr[0])), fmaf(s0[1], g0[1], bfhi(xr[0]))),
                pkbf(fmaf(s0[2], g0[2], bflo(xr[1])), fmaf(s0[3], g0[3], bfhi(xr[1]))),
                pkbf(fmaf(s1[0], g1[0], bflo(xr[2])), fmaf(s1[1], g1[1], bfhi(xr[2]))),
                pkbf(fmaf(s1[2], g1[2], bflo(xr[3])), fmaf(s1[3], g1[3], bfhi(xr[3])))};
  const int rp = 64 * rt + row;
#define OUT_AUX 16
  __builtin_amdgcn_raw_buffer_store_b128(o, __builtin_amdgcn_make_buffer_rsrc(out, 0, 2097152, 0x00020000),
                                         ((16 * (rp & 127) + (rp >> 7)) * 512 + dcol) * 2, 0, OUT_AUX);
}

DI int gemv64_col(int j) { const int sec = j >> 3; return (sec < 2 ? 1536 + 512 * sec : (sec == 2 ? 1024 : 2560)) + 64 * (j & 7); }

__global__ __launch_bounds__(512, 2) void k_fused(const u16* __restrict__ x, const u16* __restrict__ wr, const u16* __restrict__ wi,
                                                  const u16* __restrict__ wg, const u16* __restrict__ wu, const u16* __restrict__ wd,
                                                  const u16* __restrict__ cvec, const u16* __restrict__ wmod, char* ws,
                                                  u16* __restrict__ out) {
  extern __shared__ __attribute__((aligned(16))) char smem[];
  const int b = blockIdx.x, tid = threadIdx.x;
  const int w = __builtin_amdgcn_readfirstlane(tid >> 6), l = tid & 63;
  const int s2 = (b & 7) * 2 + ((b >> 3) & 1), hi = b >> 4;
  STAMP(0);
#define CODE_KB 59
  if (CODE_KB && w == 7) {
    const char* pc = (const char*)(__builtin_amdgcn_s_getpc() & ~(u64)255);
    const int kb = (b >> 3) * 2;
    glds_v(pc + min(kb, CODE_KB - 1) * 1024 + l * 16, (unsigned)(size_t)(LDS3 char*)smem + RING);
    glds_v(pc + min(kb + 1, CODE_KB - 1) * 1024 + l * 16, (unsigned)(size_t)(LDS3 char*)smem + RING + 1024);
  }
  asm volatile("" :: "s"(x), "s"(wr), "s"(wi), "s"(wg), "s"(wu), "s"(wd), "s"(cvec), "s"(wmod), "s"(ws), "s"(out));
  __builtin_amdgcn_sched_barrier(0);
  const unsigned tag = *(__attribute__((address_space(4))) const unsigned*)(uintptr_t)(ws + OFF_EPOCH) + 1;
  u4 ld[16];
#define GV_NAP 6
  if (__builtin_expect(b < 128, 1)) {
    if (w < 4) p0_load(x, ld, s2, hi, w, l);
  } else if (b < 193) {
    p1_load(wr, wi, ld, b - 128, tid);
  } else if (b < 225) {
    gemv_load<32>(cvec, wmod, ld, 32 * (b - 193), tid);
  } else {
    for (int i = 0; i < GV_NAP; ++i) __builtin_amdgcn_s_sleep(4);
    gemv_load<64>(cvec, wmod, ld, gemv64_col(b - 225), tid);
  }
  asm volatile("" ::: "memory");
  if (__builtin_expect(b < 128, 1)) phase0(x, ld, ws, smem, s2, hi, w, l);
  else if (b >= 225) mod_gemv<64>(ld, ws, smem, tag, gemv64_col(b - 225), tid);
  else if (b >= 193) mod_gemv<32>(ld, ws, smem, tag, 32 * (b - 193), tid);
  P2Pre P2;
  if (b < 128) {
    STAMP(3);
    publish(ws, OFF_FA + b * FS, tag);
    STAMP(4);
    phase2_pre(x, P2, s2, hi, w, l);
#define PRE_NAP 0
    for (int i = 0; i < PRE_NAP; ++i) __builtin_amdgcn_s_sleep(4);
  } else if (b < 193) {
    phase1(ld, ws, smem, tag, b - 128, w, l);
    publish(ws, OFF_FB + (b - 128) * FS, tag);
  } else {
    STAMP(1);
    if (b >= 225) publish(ws, OFF_FM + (b - 193) * FS, tag);
    else bar();
    if (b == 236) {
      gemv_load<64>(cvec, wmod, ld, gemv64_col(31), tid);
      mod_gemv<64>(ld, ws, smem, tag, gemv64_col(31), tid);
      publish(ws, OFF_FM + 63 * FS, tag);
    } else if (b < 236) {
      pq_job(wg, wu, ws, smem, tag, b - 193, w, l);
      publish(ws, OFF_FQ + (b - 193) * FS, tag);
    }
  }
  if (tid == 64) __hip_atomic_store((gu32*)(ws + OFF_ALIVE + b * 4), tag, RLX);
  STAMP(7);

  const int rt = 2 * s2 + (hi & 1), ct = hi >> 1;
  const int chunk = 4 * (b & 3) + ((b >> 3) & 3), panel = 8 * ((b >> 2) & 1) + (b >> 5);
  Loader L;
  {
    L.lw = w & 3;
    L.lds = (unsigned)(size_t)(LDS3 char*)smem + L.lw * 3072;
#pragma unroll
    for (int j = 0; j < 3; ++j) {
      const int i = 64 * j + l, row = i / 24, r = i - 24 * row, mat = r >= 12;
      const int piece = (r - 12 * mat) ^ (row & 2);
      L.p1[j] = (const char*)((mat ? wu : wg) + (size_t)(8 * L.lw + row) * FH + min(96 * chunk + 8 * piece, FH - 8));
    }
    L.voff2 = (l >> 3) * 1024 + (((l & 7) ^ ((l >> 2) & 4)) * 16);
    L.w2 = (const char*)wd + ct * 128;
  }
  u4 wdir[4];
  {
    const int ks = w2_step(19 + 20 * (w >> 1) - (w >> 2) * ((w >> 1) - 1)) + 1 + (w & 1);
    const unsigned dst = (unsigned)(size_t)(LDS3 char*)smem + P2_IMG + w * 2048;
    glds_s(L.voff2, L.w2 + ks * 16384, dst);
    glds_s(L.voff2, L.w2 + ks * 16384 + 8192, dst + 1024);
    if (w >= 4 && chunk < 15) {
#pragma unroll
      for (int s = 0; s < NPRE; ++s) load_w1(L, s);
      waitv<3 * NPRE>();
    } else {
      waitv<0>();
    }
    if (w == 1) {
      poll<FS, 4>(ws, OFF_FM + 40 * FS, 24, tag, l);
      const int dm = 32 * (8 * (hi & 1) + (l >> 3)) + 8 * ((hi >> 1) & 3) + (l & 7);
      *(unsigned*)(smem + P2_MOD + l * 4) = ld4(ws, OFF_MOD + (1024 + dm) * 4);
      *(unsigned*)(smem + P2_MOD + 256 + l * 4) = ld4(ws, OFF_MOD + (2048 + dm) * 4);
    } else if (w == 2 && l < 8) {
      *(unsigned*)(smem + SYNC + l * 4) = 0u;
    }
    bar();
    const char* p = smem + P2_IMG + (((w >> 1) + ct) & 3) * 4096 + w2_lane(l);
    w2_frags(p, (l & 8) * 8, wdir[0], wdir[1]);
    w2_frags(p + 2048, (l & 8) * 8, wdir[2], wdir[3]);
    bar();
  }
  STAMP(11);
  if (b < 128) {
    phase2(P2, ws, smem, tag, s2, hi, w, l);
  }
  STAMP(12);
  if (chunk < 15) {
    if (w < 4) {
      if (chunk < 14) g1_compute<6>(ws, smem, tag, panel, chunk, w, l);
      else g1_compute<2>(ws, smem, tag, panel, chunk, w, l);
    } else {
      if (w == 5) poll<FS, 2>(ws, OFF_FQ, 43, tag, l);
      waitv<0>();
      load_w1(L, 11);
      load_w1(L, 12);
      bar();
      if (tid == 256 && b < 128) __hip_atomic_store((gu32*)(ws + OFF_FX + (s2 * 8 + hi) * FS), tag, RLX);
      unsigned seen = 0;
      g1_load_step<0>(L, smem, l, seen);
    }
    bar();
    if (tid == 0) __hip_atomic_store((gu32*)(ws + OFF_FD + (panel * 16 + chunk) * FS), tag, RLX);
    STAMP(16);
    if (w == 1) poll<FS>(ws, OFF_FX + s2 * 8 * FS, 8, tag, l);
  } else {
    if (w >= 4) {
#pragma unroll
      for (int p = 0; p < NSLOT; ++p) load_w2(L, p);
    }
    if (b == 255 && w == 0) {
      poll<4, 8>(ws, OFF_ALIVE, 128, tag, l);
      poll<4, 8>(ws, OFF_ALIVE + 512, 128, tag, l);
      if (l == 0) __hip_atomic_store((gu32*)(ws + OFF_EPOCH), tag, RLX);
    }
    if (b < 128) publish(ws, OFF_FX + (s2 * 8 + hi) * FS, tag);
    if (w == 1) poll<FS, 4>(ws, OFF_FX + s2 * 8 * FS, 8, tag, l);
  }
  gemm2(ws, smem, out, wdir, tag, rt, ct, w, l);
}

extern "C" void kernel_launch(void* const* d_in, const int* in_sizes, int n_in, void* d_out, int out_size,
                              void* d_ws, size_t ws_size, hipStream_t stream) {
  (void)hipFuncSetAttribute((const void*)k_fused, hipFuncAttributeMaxDynamicSharedMemorySize, LDS_BYTES);
  k_fused<<<256, 512, LDS_BYTES, stream>>>((const u16*)d_in[0], (const u16*)d_in[1], (const u16*)d_in[2], (const u16*)d_in[4],
                                           (const u16*)d_in[5], (const u16*)d_in[6], (const u16*)d_in[7], (const u16*)d_in[8],
                                           (char*)d_ws, (u16*)d_out);
}
```
